```python
import torch

def _softmax(x, axis=-1):
    return torch.softmax(x, dim=axis)
def _gelu(x):
    return torch.nn.functional.gelu(x, approximate="tanh")
def _silu(x):
    return torch.nn.functional.silu(x)
def _sigmoid(x):
    return torch.sigmoid(x)
def _layernorm(x, axis=-1, eps=1e-5):
    m = x.mean(dim=axis, keepdim=True)
    v = x.var(dim=axis, keepdim=True, unbiased=False)
    return (x - m) / torch.sqrt(v + eps)
def _scatter_add(tgt, idx, src, axis=0):
    return tgt.clone().index_add_(axis, idx, src)
def _segment_sum(src, seg, n):
    out = torch.zeros((n,) + src.shape[1:], dtype=src.dtype)
    return out.index_add_(0, seg, src)
def _pad(x, pw):
    flat = tuple(v for pair in reversed(pw) for v in pair)
    return torch.nn.functional.pad(x, flat)

kernel_name = 'parallel_sum__ssm_conv_s4d__moe_arch_mod__prompt'

def setup_inputs():

    _w = (lambda a: (lambda t: t.to(torch.bfloat16) if t.is_floating_point() else t)(torch.as_tensor(a)))
    def _A_setup():
        Bsz, L, D, N = 1, 2048, 1024, 128
        return {
            "u": _w((lambda *s: torch.randn(*s, dtype=torch.bfloat16) * 0.05)(Bsz, L, D)),
            "log_A_real": _w((lambda *s: torch.randn(*s, dtype=torch.bfloat16) * 3.2)(D, N) * 0.5),
            "A_imag": _w((lambda *s: torch.randn(*s, dtype=torch.bfloat16) * 0.2)(D, N) * 3.14),
            "C_real": _w((lambda *s: torch.randn(*s, dtype=torch.bfloat16) * 3.2)(D, N) / N ** 0.5),
            "C_imag": _w((lambda *s: torch.randn(*s, dtype=torch.bfloat16) * 3.2)(D, N) / N ** 0.5),
            "log_dt": _w((lambda *s: torch.randn(*s, dtype=torch.bfloat16) * 0.2)(D) * 0.5 - 4.0),
            "D_skip": _w((lambda *s: torch.randn(*s, dtype=torch.bfloat16) * 0.4)(D)),
        }
    _A_in = _A_setup()

    def _B_setup():
        B, S, D, F, C = 1, 2048, 1024, 3584, 1
        return {
            "x": _w((lambda *s: torch.randn(*s, dtype=torch.bfloat16) * 0.05)(B, S, D)),
            "w_r": _w((lambda *s: torch.randn(*s, dtype=torch.bfloat16) * 0.05)(D)),
            "w_gate": _w((lambda *s: torch.randn(*s, dtype=torch.bfloat16) * 0.05)(D, F)),
            "w_up": _w((lambda *s: torch.randn(*s, dtype=torch.bfloat16) * 0.05)(D, F)),
            "w_down": _w((lambda *s: torch.randn(*s, dtype=torch.bfloat16) * 0.05)(F, D)),
        }
    _B_in = _B_setup()

    return {
        "A_u": _A_in["u"],
        "A_log_A_real": _A_in["log_A_real"],
        "A_A_imag": _A_in["A_imag"],
        "A_C_real": _A_in["C_real"],
        "A_C_imag": _A_in["C_imag"],
        "A_log_dt": _A_in["log_dt"],
        "A_D_skip": _A_in["D_skip"],
        "B_x": _B_in["x"],
        "B_w_r": _B_in["w_r"],
        "B_w_gate": _B_in["w_gate"],
        "B_w_up": _B_in["w_up"],
        "B_w_down": _B_in["w_down"],
    }

def reference(A_u, A_log_A_real, A_A_imag, A_C_real, A_C_imag, A_log_dt, A_D_skip, B_x, B_w_r, B_w_gate, B_w_up, B_w_down):
    def _A_ref(u, log_A_real, A_imag, C_real, C_imag, log_dt, D_skip):
        L = u.shape[1]
        dt = torch.exp(log_dt)
        A = -torch.exp(log_A_real) + 1j * A_imag
        C = (C_real + 1j * C_imag) * dt[:, None]
        pos = (lambda x, dt: x.to(dt))(torch.arange(L), log_dt.dtype)
        P = torch.exp((A * dt[:, None])[:, :, None] * pos[None, None, :])
        K = 2.0 * torch.einsum("dn,dnl->dl", C, P).real
        L2 = 2 * L
        u_t = (lambda x, *d: x.permute(*d))(u, 0, 2, 1)
        Kf = (lambda x, *a, **k: torch.fft.rfft(x.float(), *a, **k))(K, L2)
        Uf = (lambda x, *a, **k: torch.fft.rfft(x.float(), *a, **k))(u_t, L2)
        y = (lambda x, *a, **k: torch.fft.irfft(x, *a, **k).to(torch.bfloat16))(Uf * Kf[None, :, :], L2)[:, :, :L]
        y = (lambda x, *d: x.permute(*d))(y, 0, 2, 1)
        return y + u * D_skip[None, None, :]

    def _B_ref(x, w_r, w_gate, w_up, w_down):
        B, S, D = x.shape
        C = 1
        r = x @ w_r
        idx = torch.argsort(-r, axis=-1)[:, :C]
        idx_e = (lambda x, n, axis: torch.repeat_interleave(x, n, dim=axis))(idx.reshape(B, C, 1), D, axis=2)
        x_sel = (lambda x, i, axis: torch.take_along_dim(x, i, dim=axis))(x, idx_e, axis=1)
        g = _silu(x_sel @ w_gate)
        u = x_sel @ w_up
        y = (g * u) @ w_down
        r_sel = _sigmoid((lambda x, i, axis: torch.take_along_dim(x, i, dim=axis))(r, idx, axis=1))
        y = y * r_sel.reshape(B, C, 1)
        b_off = (torch.arange(B) * S).reshape(B, 1)
        flat_idx = (idx + b_off).reshape(B * C)
        zero = (x * 0.0).reshape(B * S, D)
        upd = _scatter_add(zero, flat_idx, y.reshape(B * C, D), axis=0)
        return x + upd.reshape(B, S, D)

    x = A_u
    xn = _layernorm(x, axis=-1)
    return x + _A_ref(xn, A_log_A_real, A_A_imag, A_C_real, A_C_imag, A_log_dt, A_D_skip) + _B_ref(xn, B_w_r, B_w_gate, B_w_up, B_w_down)
```

```cpp
#include <hip/hip_runtime.h>
#include <hip/hip_bf16.h>
#include <stdint.h>

typedef unsigned short u16;
constexpr int L = 2048, D = 1024, NM = 128, F = 3584;

__device__ __forceinline__ float bf2f(u16 v) { return __uint_as_float(((unsigned)v) << 16); }
__device__ __forceinline__ u16 f2bf(float f) {
    unsigned u = __float_as_uint(f);
    if ((u & 0x7fffffffu) > 0x7f800000u) return (u16)((u >> 16) | 0x40);
    return (u16)((u + 0x7FFFu + ((u >> 16) & 1u)) >> 16);
}
__device__ __forceinline__ float rbf(float f) { return bf2f(f2bf(f)); }

__global__ void k_ln(const u16* x, const u16* wr, u16* xn, float* r) {
    int row = blockIdx.x; int t = threadIdx.x;
    __shared__ float red[256];
    float v[4]; float s = 0;
    for (int i = 0; i < 4; i++) { v[i] = bf2f(x[row * D + t * 4 + i]); s += v[i]; }
    red[t] = s; __syncthreads();
    for (int o = 128; o > 0; o >>= 1) { if (t < o) red[t] += red[t + o]; __syncthreads(); }
    float mean = red[0] / D; __syncthreads();
    float q = 0; for (int i = 0; i < 4; i++) { float dd = v[i] - mean; q += dd * dd; }
    red[t] = q; __syncthreads();
    for (int o = 128; o > 0; o >>= 1) { if (t < o) red[t] += red[t + o]; __syncthreads(); }
    float var = red[0] / D; __syncthreads();
    float m = rbf(mean), vb = rbf(var);
    float sd = rbf(sqrtf(rbf(vb + 1e-5f)));
    float acc = 0;
    for (int i = 0; i < 4; i++) {
        float n = rbf(rbf(v[i] - m) / sd);
        xn[row * D + t * 4 + i] = f2bf(n);
        acc += n * bf2f(wr[t * 4 + i]);
    }
    red[t] = acc; __syncthreads();
    for (int o = 128; o > 0; o >>= 1) { if (t < o) red[t] += red[t + o]; __syncthreads(); }
    if (t == 0) r[row] = rbf(red[0]);
}

__global__ void k_kgen(const u16* lar, const u16* aim, const u16* cre, const u16* cim, const u16* ldt, float* K) {
    int d = blockIdx.x; int t = threadIdx.x;
    __shared__ float ar[NM], ai[NM], cr[NM], ci[NM];
    float dt = rbf(expf(bf2f(ldt[d])));
    if (t < NM) {
        ar[t] = -rbf(expf(bf2f(lar[d * NM + t]))) * dt;
        ai[t] = bf2f(aim[d * NM + t]) * dt;
        cr[t] = bf2f(cre[d * NM + t]) * dt;
        ci[t] = bf2f(cim[d * NM + t]) * dt;
    }
    __syncthreads();
    for (int l = t; l < L; l += 256) {
        float p = rbf((float)l);
        float acc = 0;
        for (int n = 0; n < NM; n++) {
            float e = expf(ar[n] * p); float s, c; sincosf(ai[n] * p, &s, &c);
            acc += cr[n] * e * c - ci[n] * e * s;
        }
        K[d * L + l] = 2.0f * acc;
    }
}

__global__ void k_conv(const u16* xn, const float* K, float* y) {
    int d = blockIdx.x; int t = threadIdx.x;
    __shared__ float ks[L], us[L];
    for (int l = t; l < L; l += 256) { ks[l] = K[d * L + l]; us[l] = bf2f(xn[l * D + d]); }
    __syncthreads();
    for (int l = t; l < L; l += 256) {
        float acc = 0;
        for (int j = 0; j <= l; j++) acc += ks[j] * us[l - j];
        y[d * L + l] = acc;
    }
}

__global__ void k_gu(const float* r, const u16* xn, const u16* wg, const u16* wu, float* h, int* idxo) {
    __shared__ float bv[256]; __shared__ int bi[256]; __shared__ float xs[D];
    int t = threadIdx.x;
    float best = -INFINITY; int bidx = 0;
    for (int l = t; l < L; l += 256) { float v = r[l]; if (v > best) { best = v; bidx = l; } }
    bv[t] = best; bi[t] = bidx; __syncthreads();
    for (int o = 128; o > 0; o >>= 1) {
        if (t < o) { if (bv[t + o] > bv[t] || (bv[t + o] == bv[t] && bi[t + o] < bi[t])) { bv[t] = bv[t + o]; bi[t] = bi[t + o]; } }
        __syncthreads();
    }
    int idx = bi[0];
    for (int i = t; i < D; i += 256) xs[i] = bf2f(xn[idx * D + i]);
    __syncthreads();
    int f = blockIdx.x * 256 + t;
    float g = 0, u = 0;
    for (int k = 0; k < D; k++) { g += xs[k] * bf2f(wg[k * F + f]); u += xs[k] * bf2f(wu[k * F + f]); }
    g = rbf(g); u = rbf(u);
    float sg = rbf(g / (1.0f + expf(-g)));
    h[f] = rbf(sg * u);
    if (f == 0) *idxo = idx;
}
__global__ void k_down(const float* h, const u16* wd, const float* r, const int* idxp, float* upd) {
    int d = blockIdx.x * 64 + (threadIdx.x & 63); int part = threadIdx.x >> 6;
    __shared__ float red[4][64];
    float acc = 0;
    for (int f = part; f < F; f += 4) acc += h[f] * bf2f(wd[f * D + d]);
    red[part][threadIdx.x & 63] = acc; __syncthreads();
    if (part == 0) {
        float y = rbf(red[0][threadIdx.x] + red[1][threadIdx.x] + red[2][threadIdx.x] + red[3][threadIdx.x]);
        float rs = rbf(1.0f / (1.0f + expf(-r[*idxp])));
        upd[d] = rbf(y * rs);
    }
}
__global__ void k_fin(const u16* x, const u16* xn, const float* y, const u16* dsk, const float* upd, const int* idxp, u16* out) {
    int i = blockIdx.x * 256 + threadIdx.x; int l = i / D, d = i % D;
    float xv = bf2f(x[i]), n = bf2f(xn[i]);
    float a = rbf(rbf(y[d * L + l]) + rbf(n * bf2f(dsk[d])));
    float b = (l == *idxp) ? rbf(n + upd[d]) : n;
    out[i] = f2bf(rbf(xv + a) + b);
}

extern "C" void kernel_launch(void* const* d_in, const int* in_sizes, int n_in,
                              void* d_out, int out_size, void* d_ws, size_t ws_size,
                              hipStream_t stream) {
    const u16* x = (const u16*)d_in[0];
    const u16* lar = (const u16*)d_in[1]; const u16* aim = (const u16*)d_in[2];
    const u16* cre = (const u16*)d_in[3]; const u16* cim = (const u16*)d_in[4];
    const u16* ldt = (const u16*)d_in[5]; const u16* dsk = (const u16*)d_in[6];
    const u16* wr = (const u16*)d_in[8]; const u16* wg = (const u16*)d_in[9];
    const u16* wu = (const u16*)d_in[10]; const u16* wd = (const u16*)d_in[11];
    char* ws = (char*)d_ws;
    u16* xn = (u16*)ws; ws += L * D * 2;
    float* K = (float*)ws; ws += L * D * 4;
    float* y = (float*)ws; ws += L * D * 4;
    float* r = (float*)ws; ws += L * 4;
    float* h = (float*)ws; ws += F * 4;
    float* upd = (float*)ws; ws += D * 4;
    int* idx = (int*)ws;
    k_ln<<<L, 256, 0, stream>>>(x, wr, xn, r);
    k_kgen<<<D, 256, 0, stream>>>(lar, aim, cre, cim, ldt, K);
    k_conv<<<D, 256, 0, stream>>>(xn, K, y);
    k_gu<<<F / 256, 256, 0, stream>>>(r, xn, wg, wu, h, idx);
    k_down<<<D / 64, 256, 0, stream>>>(h, wd, r, idx, upd);
    k_fin<<<L * D / 256, 256, 0, stream>>>(x, xn, y, dsk, upd, idx, (u16*)d_out);
}
```

```cpp
#include <hip/hip_runtime.h>
#include <stdint.h>

typedef unsigned short u16;
typedef unsigned v4u __attribute__((ext_vector_type(4)));
typedef unsigned v2u __attribute__((ext_vector_type(2)));
typedef float f4 __attribute__((ext_vector_type(4)));
typedef float f2 __attribute__((ext_vector_type(2)));
typedef float f16v __attribute__((ext_vector_type(16)));
typedef _Float16 h8 __attribute__((ext_vector_type(8)));
typedef _Float16 h2 __attribute__((ext_vector_type(2)));
typedef __bf16 b2 __attribute__((ext_vector_type(2)));
#define DI __device__ __forceinline__
#define AUX_NT 2
#define AUX_SC1 16

constexpr int LSEQ = 2048, DM = 1024, FF = 3584;
constexpr unsigned WS_ST = 0;
constexpr unsigned WS_CAND = 8192;
constexpr unsigned WS_GU = 16384;
constexpr unsigned WS_P = WS_GU + 224 * 512;
constexpr unsigned WS_HOP_BYTES = 224 * 512 + 56 * 4096;
constexpr unsigned WS_XT = 1u << 20;
constexpr unsigned WS_BYTES = WS_XT + (4u << 20);
constexpr unsigned SPIN_CAP = 1u << 18;

DI float bflo(unsigned v) { return __uint_as_float(v << 16); }
DI float bfhi(unsigned v) { return __uint_as_float(v & 0xffff0000u); }
DI unsigned pkbf(float a, float b) { b2 v = {(__bf16)a, (__bf16)b}; return __builtin_bit_cast(unsigned, v); }
DI float rbf(float a) { return bflo(pkbf(a, a)); }
DI unsigned pkh(float a, float b) { h2 v = {(_Float16)a, (_Float16)b}; return __builtin_bit_cast(unsigned, v); }
DI float dpp_ror4(float x) { return __int_as_float(__builtin_amdgcn_update_dpp(0, __float_as_int(x), 0x124, 0xf, 0xf, false)); }
DI float dpp_ror8(float x) { return __int_as_float(__builtin_amdgcn_update_dpp(0, __float_as_int(x), 0x128, 0xf, 0xf, false)); }
DI float dpp_ror1(float x) { return __int_as_float(__builtin_amdgcn_update_dpp(0, __float_as_int(x), 0x121, 0xf, 0xf, false)); }
DI float dpp_ror2(float x) { return __int_as_float(__builtin_amdgcn_update_dpp(0, __float_as_int(x), 0x122, 0xf, 0xf, false)); }
DI f2 swap16(float a, float b) { auto r = __builtin_amdgcn_permlane16_swap(__float_as_uint(a), __float_as_uint(b), false, false); return (f2){__uint_as_float(r[0]), __uint_as_float(r[1])}; }
DI f2 swap32(float a, float b) { auto r = __builtin_amdgcn_permlane32_swap(__float_as_uint(a), __float_as_uint(b), false, false); return (f2){__uint_as_float(r[0]), __uint_as_float(r[1])}; }
DI float sum16(float a) { f2 r = swap16(a, a); return r.x + r.y; }
DI float sum32(float a) { f2 r = swap32(a, a); return r.x + r.y; }
DI float wave_sum(float v) { v += dpp_ror1(v); v += dpp_ror2(v); v += dpp_ror4(v); v += dpp_ror8(v); v = sum16(v); return sum32(v); }
DI float wave_max(float v) {
    v = fmaxf(v, dpp_ror1(v)); v = fmaxf(v, dpp_ror2(v)); v = fmaxf(v, dpp_ror4(v)); v = fmaxf(v, dpp_ror8(v));
    f2 r = swap16(v, v); v = fmaxf(r.x, r.y); r = swap32(v, v); return fmaxf(r.x, r.y);
}
DI unsigned wave_umax(unsigned v) {
    v = max(v, (unsigned)__builtin_amdgcn_update_dpp(0, (int)v, 0x121, 0xf, 0xf, false));
    v = max(v, (unsigned)__builtin_amdgcn_update_dpp(0, (int)v, 0x122, 0xf, 0xf, false));
    v = max(v, (unsigned)__builtin_amdgcn_update_dpp(0, (int)v, 0x124, 0xf, 0xf, false));
    v = max(v, (unsigned)__builtin_amdgcn_update_dpp(0, (int)v, 0x128, 0xf, 0xf, false));
    auto r = __builtin_amdgcn_permlane16_swap(v, v, false, false); v = max(r[0], r[1]);
    r = __builtin_amdgcn_permlane32_swap(v, v, false, false); return max(r[0], r[1]);
}
DI __amdgpu_buffer_rsrc_t mkrs(const void* p, unsigned bytes) { return __builtin_amdgcn_make_buffer_rsrc((void*)p, 0, bytes, 0x00020000); }

__global__ void __launch_bounds__(256) k1_ln(const u16* __restrict__ x, const u16* __restrict__ wr, char* __restrict__ ws) {
    __shared__ f2 sst[4][8];
    __shared__ float srr[4][8];
    const int b = blockIdx.x, tid = threadIdx.x, lane = tid & 63, wave = tid >> 6;
    const int g = b >> 2, j = b & 3, a = lane >> 2, i = lane & 3;
    const int R0 = 1024 * (j >> 1) + 16 * g + 4 * i + 2 * (j & 1);
    auto rx = mkrs(x, LSEQ * DM * 2);
    auto rw = mkrs(wr, DM * 2);
    auto rws = mkrs(ws, WS_BYTES);
    v4u v[2][2], w[2];
#pragma unroll
    for (int rr = 0; rr < 2; rr++)
#pragma unroll
        for (int kk = 0; kk < 2; kk++) v[rr][kk] = __builtin_amdgcn_raw_buffer_load_b128(rx, (R0 + rr) * 2048 + (a + 16 * (2 * wave + kk)) * 16, 0, AUX_NT);
#pragma unroll
    for (int kk = 0; kk < 2; kk++) w[kk] = __builtin_amdgcn_raw_buffer_load_b128(rw, (a + 16 * (2 * wave + kk)) * 16, 0, 0);
    if (tid < 84) __builtin_amdgcn_raw_buffer_store_b128((v4u){0, 0, 0, 0}, rws, WS_GU + b * 1344 + tid * 16, 0, 0);
#pragma unroll
    for (int kk = 0; kk < 2; kk++)
#pragma unroll
        for (int hh = 0; hh < 2; hh++) {
            const int cg = 2 * (a + 16 * (2 * wave + kk)) + hh;
            v4u pc = {v[0][kk][2 * hh], v[0][kk][2 * hh + 1], v[1][kk][2 * hh], v[1][kk][2 * hh + 1]};
            __builtin_amdgcn_raw_buffer_store_b128(pc, rws, WS_XT + cg * 16384 + j * 4096 + (4 * g + i) * 16, 0, 0);
        }
    float f[2][16], mean[2], m2[2];
#pragma unroll
    for (int rr = 0; rr < 2; rr++) {
#pragma unroll
        for (int kk = 0; kk < 2; kk++)
#pragma unroll
            for (int e = 0; e < 4; e++) { f[rr][kk * 8 + 2 * e] = bflo(v[rr][kk][e]); f[rr][kk * 8 + 2 * e + 1] = bfhi(v[rr][kk][e]); }
        float s = 0;
#pragma unroll
        for (int e = 0; e < 16; e++) s += f[rr][e];
        s *= (1.0f / 16);
        float q = 0;
#pragma unroll
        for (int e = 0; e < 16; e++) { float d = f[rr][e] - s; q += d * d; }
        mean[rr] = s; m2[rr] = q;
    }
#pragma unroll
    for (int rr = 0; rr < 2; rr++) {
        float m = mean[rr], q = m2[rr], mo, qo, d;
        mo = dpp_ror8(m); qo = dpp_ror8(q); d = m - mo; q = q + qo + d * d * 8.0f; m = 0.5f * (m + mo);
        mo = dpp_ror4(m); qo = dpp_ror4(q); d = m - mo; q = q + qo + d * d * 16.0f; m = 0.5f * (m + mo);
        f2 rm = swap16(m, m), rq = swap16(q, q); d = rm.x - rm.y; q = rq.x + rq.y + d * d * 32.0f; m = 0.5f * (rm.x + rm.y);
        rm = swap32(m, m); rq = swap32(q, q); d = rm.x - rm.y; q = rq.x + rq.y + d * d * 64.0f; m = 0.5f * (rm.x + rm.y);
        if (a == 0) sst[wave][2 * i + rr] = (f2){m, q};
    }
    __syncthreads();
    float racc[2]; unsigned msb[2];
#pragma unroll
    for (int rr = 0; rr < 2; rr++) {
        f2 s0 = sst[0][2 * i + rr], s1 = sst[1][2 * i + rr], s2 = sst[2][2 * i + rr], s3 = sst[3][2 * i + rr];
        float d01 = s0.x - s1.x, d23 = s2.x - s3.x;
        float ma = 0.5f * (s0.x + s1.x), mb = 0.5f * (s2.x + s3.x);
        float qa = s0.y + s1.y + d01 * d01 * 128.0f, qb = s2.y + s3.y + d23 * d23 * 128.0f;
        float dd = ma - mb;
        float mt = 0.5f * (ma + mb), qt = qa + qb + dd * dd * 256.0f;
        float mbf = rbf(mt), vbf = rbf(qt * (1.0f / 1024));
        float sd = rbf(sqrtf(rbf(vbf + 1e-5f)));
        float rinv = 1.0f / sd;
        msb[rr] = (__float_as_uint(mbf) >> 16) | (__float_as_uint(sd) & 0xffff0000u);
        float acc = 0;
#pragma unroll
        for (int kk = 0; kk < 2; kk++)
#pragma unroll
            for (int e = 0; e < 4; e++) {
                unsigned dp = pkbf(f[rr][kk * 8 + 2 * e] - mbf, f[rr][kk * 8 + 2 * e + 1] - mbf);
                unsigned np = pkbf(bflo(dp) * rinv, bfhi(dp) * rinv);
                acc += bflo(np) * bflo(w[kk][e]); acc += bfhi(np) * bfhi(w[kk][e]);
            }
        acc += dpp_ror8(acc); acc += dpp_ror4(acc); acc = sum16(acc); acc = sum32(acc);
        racc[rr] = acc;
        if (a == 0) srr[wave][2 * i + rr] = acc;
    }
    if (wave == 0 && a == 0) __builtin_amdgcn_raw_buffer_store_b64((v2u){msb[0], msb[1]}, rws, WS_ST + R0 * 4, 0, 0);
    __syncthreads();
    if (tid < 8) {
        float r = (srr[0][tid] + srr[1][tid]) + (srr[2][tid] + srr[3][tid]);
        unsigned rb = pkbf(r, r) & 0xffffu;
        unsigned key = (rb & 0x8000u) ? (~rb & 0xffffu) : (rb | 0x8000u);
        const int row = 1024 * (j >> 1) + 16 * g + 4 * (tid >> 1) + 2 * (j & 1) + (tid & 1);
        unsigned k2 = (key << 16) | (unsigned)(2047 - row);
        k2 = max(k2, (unsigned)__builtin_amdgcn_update_dpp(0, (int)k2, 0x111, 0xf, 0xf, false));
        k2 = max(k2, (unsigned)__builtin_amdgcn_update_dpp(0, (int)k2, 0x112, 0xf, 0xf, false));
        k2 = max(k2, (unsigned)__builtin_amdgcn_update_dpp(0, (int)k2, 0x114, 0xf, 0xf, false));
        if (tid == 7) __builtin_amdgcn_raw_buffer_store_b32(k2, rws, WS_CAND + b * 4, 0, 0);
    }
}

constexpr int UB = 6464;
constexpr int L_U = 0;
constexpr int KDB = 2432, KDO = 1216;
constexpr int L_KD = L_U + 4 * UB;
constexpr int FTB = 2304;
constexpr int L_FT = L_KD + 4 * KDB;
constexpr int XYB = 53 * 512;
constexpr int L_XY = L_FT + 4 * FTB;
constexpr int L_MO = L_XY + 4 * XYB;
constexpr int L_GUR = L_MO, L_PR = L_MO + 2048;
constexpr int L_CT = L_PR + 4096;
constexpr int L_TOT = L_CT + 128;
enum { C_U = 0, C_Y0 = 1, C_Y1 = 2, C_GU = 3, C_P = 4, C_IDX = 5, C_ROWF = 6, C_DUMMY = 7, C_ROWV = 8, C_OSC = 12 };

struct K2Args {
    const u16* x; const u16* lar; const u16* aim; const u16* cre; const u16* cim; const u16* ldt; const u16* dsk;
    const u16* wg; const u16* wu; const u16* wd; u16* out; char* ws;
};

DI void lds_arrive(char* lds, int word, int lane) {
    asm volatile("s_waitcnt lgkmcnt(0)" ::: "memory");
    if (lane == 0) __hip_atomic_fetch_add((unsigned*)(lds + L_CT) + word, 1u, __ATOMIC_RELAXED, __HIP_MEMORY_SCOPE_WORKGROUP);
}
DI unsigned lds_arrive_rtn(char* lds, int word, int lane) {
    asm volatile("s_waitcnt lgkmcnt(0)" ::: "memory");
    unsigned r = 0;
    if (lane == 0) r = __hip_atomic_fetch_add((unsigned*)(lds + L_CT) + word, 1u, __ATOMIC_RELAXED, __HIP_MEMORY_SCOPE_WORKGROUP);
    r = __builtin_amdgcn_readfirstlane(r);
    asm volatile("" ::: "memory");
    return r;
}
DI unsigned lds_wait_ge(char* lds, int word, unsigned want) {
    unsigned v;
    _Pragma("nounroll") for (unsigned s = 0; s < (1u << 24); s++) {
        v = __builtin_amdgcn_readfirstlane(__hip_atomic_load((unsigned*)(lds + L_CT) + word, __ATOMIC_RELAXED, __HIP_MEMORY_SCOPE_WORKGROUP));
        if (v >= want) break;
        __builtin_amdgcn_s_sleep(1);
    }
    asm volatile("" ::: "memory");
    return v;
}
struct cf { float x, y; };
DI cf cmul(cf a, cf b) { return {fmaf(a.x, b.x, -(a.y * b.y)), fmaf(a.x, b.y, a.y * b.x)}; }
DI cf cexp_p(float ar2, float air, float p) {
    float m = __builtin_amdgcn_exp2f(ar2 * p);
    float t = __builtin_amdgcn_fractf(air * p);
    return {m * __builtin_amdgcn_cosf(t), m * __builtin_amdgcn_sinf(t)};
}
DI h8 as_h8(v4u v) { return __builtin_bit_cast(h8, v); }
DI v4u ld4dw(const char* p) { const unsigned* q = (const unsigned*)p; return (v4u){q[0], q[1], q[2], q[3]}; }
DI v4u band_frag(int v0, int width) {
    unsigned long long m = ((1ull << width) - 1) << 32;
    unsigned m8 = (unsigned)(m >> (v0 + 32)) & 0xffu;
    v4u r;
#pragma unroll
    for (int k = 0; k < 4; k++) { unsigned t = m8 >> (2 * k); r[k] = ((t & 1u) | ((t & 2u) << 15)) * 0x3C00u; }
    return r;
}
#define MFMA32(a, b, c) __builtin_amdgcn_mfma_f32_32x32x16_f16((a), (b), (c), 0, 0, 0)
#define MFMA16(a, b, c) __builtin_amdgcn_mfma_f32_16x16x32_f16((a), (b), (c), 0, 0, 0)

__global__ void __launch_bounds__(512, 2) k2_main(K2Args A) {
    __shared__ __attribute__((aligned(16))) char lds[L_TOT];
    const int b = blockIdx.x, tid = threadIdx.x, lane = tid & 63;
    const int wave = __builtin_amdgcn_readfirstlane(tid >> 6);
    const int cg = (b & 7) * 32 + (b >> 3);
    auto rws = mkrs(A.ws, WS_BYTES);
    auto rout = mkrs(A.out, LSEQ * DM * 2);
    if (wave < 4) {
        const int T = tid, ch = cg * 4 + wave;
        const unsigned pl = *(const unsigned*)(A.lar + ch * 128 + 2 * lane), pa = *(const unsigned*)(A.aim + ch * 128 + 2 * lane);
        const unsigned pcr = *(const unsigned*)(A.cre + ch * 128 + 2 * lane), pci = *(const unsigned*)(A.cim + ch * 128 + 2 * lane);
        const float ldt = bflo(A.ldt[ch]);
        v4u xt[4], st[2];
#pragma unroll
        for (int j = 0; j < 4; j++) xt[j] = __builtin_amdgcn_raw_buffer_load_b128(rws, WS_XT + cg * 16384 + j * 4096 + T * 16, 0, 0);
        st[0] = __builtin_amdgcn_raw_buffer_load_b128(rws, WS_ST + T * 16, 0, 0);
        st[1] = __builtin_amdgcn_raw_buffer_load_b128(rws, WS_ST + 4096 + T * 16, 0, 0);
        const v2u dsk2 = *(const v2u*)(A.dsk + cg * 4);
        char* const U = lds + L_U + wave * UB;
        char* const KD = lds + L_KD + wave * KDB;
        char* const FT = lds + L_FT + wave * FTB;
        char* const XY = lds + L_XY + wave * XYB;
        {
            const v4u z = {0, 0, 0, 0};
            *(v4u*)(U + lane * 16) = z; if (lane < 21) *(v4u*)(U + 1024 + lane * 16) = z;
            *(v4u*)(FT + lane * 16) = z; *(v4u*)(FT + 1024 + lane * 16) = z; if (lane < 16) *(v4u*)(FT + 2048 + lane * 16) = z;
            if (lane < 24) { int o = lane < 12 ? lane : lane - 12; int off = (o < 4 ? o * 16 : 1088 + (o - 4) * 16) + (lane < 12 ? 0 : KDO); *(v4u*)(KD + off) = z; }
        }
        __builtin_amdgcn_s_barrier();
        const int l31 = lane & 31, hh = lane >> 5, l15 = lane & 15, g4 = lane >> 4;
        float osc;
        {
            const float dt = rbf(expf(ldt));
            const float LOG2E = 1.4426950408889634f, INV2PI = 0.15915494309189535f;
            float ar2[2], air[2]; cf C[2];
            ar2[0] = (-rbf(expf(bflo(pl))) * dt) * LOG2E; ar2[1] = (-rbf(expf(bfhi(pl))) * dt) * LOG2E;
            air[0] = (bflo(pa) * dt) * INV2PI; air[1] = (bfhi(pa) * dt) * INV2PI;
            float mx = fmaxf(fabsf(bflo(pcr)) + fabsf(bflo(pci)), fabsf(bfhi(pcr)) + fabsf(bfhi(pci))) * (2.0f * dt);
            mx = wave_max(mx);
            int E = (__float_as_uint(mx) >> 23) & 0xff; E = min(max(E, 20), 240);
            const float sc = __uint_as_float((unsigned)(267 - E) << 23);
            osc = __uint_as_float((unsigned)(E - 14) << 23);
            const float cs = 2.0f * sc;
            C[0] = {bflo(pcr) * dt * cs, bflo(pci) * dt * cs}; C[1] = {bfhi(pcr) * dt * cs, bfhi(pci) * dt * cs};
            const int sl = lane >> 1, hb = (lane & 1) * 8;
            auto putX = [&](int row, cf a0, cf a1) { *(v2u*)(XY + row * 512 + ((sl ^ (row & 15)) << 4) + hb) = (v2u){pkh(a0.x, -a0.y), pkh(a1.x, -a1.y)}; };
            auto putY = [&](int row, cf a0, cf a1) { *(v2u*)(XY + (21 + row) * 512 + ((sl ^ (row & 15)) << 4) + hb) = (v2u){pkh(a0.x, a0.y), pkh(a1.x, a1.y)}; };
            cf z[2], w[2], zb[2], z2[2];
#pragma unroll
            for (int m = 0; m < 2; m++) { z[m] = cexp_p(ar2[m], air[m], 1.0f); w[m] = cexp_p(ar2[m], air[m], 8.0f); zb[m] = cexp_p(ar2[m], air[m], 256.0f); z2[m] = cexp_p(ar2[m], air[m], 2.0f); }
            {
                cf y[4][2];
#pragma unroll
                for (int m = 0; m < 2; m++) { y[0][m] = {1.0f, 0.0f}; y[1][m] = cexp_p(ar2[m], air[m], 64.0f); y[2][m] = cexp_p(ar2[m], air[m], 128.0f); y[3][m] = cexp_p(ar2[m], air[m], 192.0f); }
#pragma unroll
                for (int s = 0; s < 8; s++) {
#pragma unroll
                    for (int c = 0; c < 4; c++) {
                        putY(8 * c + s, y[c][0], y[c][1]);
                        if (s < 7) { y[c][0] = cmul(y[c][0], w[0]); y[c][1] = cmul(y[c][1], w[1]); }
                    }
                }
            }
            {
                cf t[2] = {C[0], C[1]};
#pragma unroll
                for (int e = 0; e < 8; e++) { putX(e, t[0], t[1]); if (e < 7) { t[0] = cmul(t[0], z[0]); t[1] = cmul(t[1], z[1]); } }
                cf p256[2] = {cmul(C[0], zb[0]), cmul(C[1], zb[1])};
                cf p258[2] = {cmul(p256[0], z2[0]), cmul(p256[1], z2[1])};
                cf p260[2] = {cmul(p258[0], z2[0]), cmul(p258[1], z2[1])};
                cf p262[2] = {cmul(p260[0], z2[0]), cmul(p260[1], z2[1])};
                putX(8, p256[0], p256[1]); putX(9, p258[0], p258[1]); putX(10, p260[0], p260[1]); putX(11, p262[0], p262[1]);
                cf p512[2] = {cmul(p256[0], zb[0]), cmul(p256[1], zb[1])};
                cf p516[2] = {cmul(p260[0], zb[0]), cmul(p260[1], zb[1])};
                cf p768[2] = {cmul(p512[0], zb[0]), cmul(p512[1], zb[1])};
                cf p772[2] = {cmul(p516[0], zb[0]), cmul(p516[1], zb[1])};
                putX(12, p512[0], p512[1]); putX(13, p516[0], p516[1]); putX(14, p768[0], p768[1]); putX(15, p772[0], p772[1]);
                t[0] = p768[0]; t[1] = p768[1];
#pragma unroll
                for (int e = 0; e < 5; e++) { t[0] = cmul(t[0], zb[0]); t[1] = cmul(t[1], zb[1]); putX(16 + e, t[0], t[1]); }
            }
        }
        f16v kacc;
#pragma unroll
        for (int e = 0; e < 16; e++) kacc[e] = 0.0f;
        {
            const int lp = hh ^ (l31 & 15);
#pragma unroll
            for (int t = 0; t < 16; t++) {
                const int so = ((lp ^ ((2 * t) & 15)) + ((2 * t) & 16)) << 4;
                v4u xa = *(const v4u*)(XY + l31 * 512 + so);
                v4u yb = *(const v4u*)(XY + (21 + l31) * 512 + so);
                kacc = MFMA32(as_h8(xa), as_h8(yb), kacc);
            }
        }
        float base[8][4];
        {
            const float d1[4] = {(1.0f + bflo(dsk2.x)) * (1.0f / 128), (1.0f + bfhi(dsk2.x)) * (1.0f / 128), (1.0f + bflo(dsk2.y)) * (1.0f / 128), (1.0f + bfhi(dsk2.y)) * (1.0f / 128)};
            float un[8][4];
#pragma unroll
            for (int r = 0; r < 8; r++) {
                const unsigned ms = st[r >> 2][r & 3];
                const float m = bflo(ms), rinv = __builtin_amdgcn_rcpf(bfhi(ms)) * 128.0f;
#pragma unroll
                for (int p = 0; p < 2; p++) {
                    const unsigned xv = xt[r >> 1][(r & 1) * 2 + p];
                    const float x0 = bflo(xv), x1 = bfhi(xv);
                    const unsigned dp = pkbf(x0 - m, x1 - m);
                    const unsigned np = pkbf(bflo(dp) * rinv, bfhi(dp) * rinv);
                    un[r][2 * p] = bflo(np); un[r][2 * p + 1] = bfhi(np);
                    base[r][2 * p] = fmaf(un[r][2 * p], d1[2 * p], x0); base[r][2 * p + 1] = fmaf(un[r][2 * p + 1], d1[2 * p + 1], x1);
                }
            }
#pragma unroll
            for (int hf = 0; hf < 2; hf++) {
                const int qp = (T >> 1) + 128 * hf + 68;
                const int off = (qp + (qp >> 2)) * 16 + ((T & 1) ? 0 : 8);
#pragma unroll
                for (int c = 0; c < 4; c++)
                    *(v2u*)(lds + L_U + c * UB + off) = (v2u){pkh(un[4 * hf + 3][c], un[4 * hf + 2][c]), pkh(un[4 * hf + 1][c], un[4 * hf][c])};
            }
        }
        lds_arrive(lds, C_U, lane);
        unsigned k1024;
        {
            float kv[12];
#pragma unroll
            for (int e = 0; e < 12; e++) kv[e] = kacc[e] * (1.0f / 64);
            const unsigned p01 = pkh(kv[0], kv[1]), p23 = pkh(kv[2], kv[3]), p12 = pkh(kv[1], kv[2]);
            const int o1 = 64 + 16 * l31 + 8 * hh;
            *(v2u*)(KD + o1) = (v2u){p01, p23};
            *(u16*)(KD + KDO + o1 - 2) = (u16)p01;
            *(unsigned*)(KD + KDO + o1) = p12;
            *(u16*)(KD + KDO + o1 + 4) = (u16)(p23 >> 16);
            const float vn = __shfl_down(kv[4], 1);
            if (hh == 0) {
                *(v4u*)(KD + 576 + 16 * l31) = (v4u){pkh(kv[4], kv[4]), pkh(kv[5], kv[6]), pkh(kv[6], kv[6]), pkh(kv[7], vn)};
                *(v4u*)(KD + KDO + 576 + 16 * l31) = (v4u){pkh(kv[4], kv[5]), pkh(kv[6], kv[6]), pkh(kv[6], kv[7]), pkh(vn, vn)};
            }
            if (lane == 0) *(u16*)(KD + KDO + 574) = (u16)pkh(kv[4], kv[4]);
            if (lane == 32) {
                *(v2u*)(KD + 1088) = (v2u){pkh(kv[4], kv[4]), pkh(kv[4], 0.0f)};
                *(unsigned*)(KD + KDO + 1088) = pkh(kv[4], kv[4]);
                *(u16*)(FT + 5 * 384) = (u16)pkh(kv[8], kv[8]);
            }
            const int bo = l31 & 1;
            int a0, a1, st2;
            if (hh) { a0 = (bo ? 1 : 3) * 384 + 2 * (79 - (31 + ((l31 + 1) >> 1))); a1 = (bo ? 2 : 0) * 384 + 2 * (79 - (32 + (l31 >> 1))); st2 = 32; }
            else { a0 = (bo ? 4 : 5) * 384 + 2 * (127 - (63 + ((l31 + 1) >> 1))); a1 = a0 - 32; st2 = 64; }
            int ad[4] = {a0, a1, a0 - st2, a1 - st2};
            if (lane == 0) ad[0] = 3 * 384 + 2 * 16;
            if (lane == 32) ad[0] = 3 * 384 + 2 * 70;
#pragma unroll
            for (int e = 0; e < 4; e++) {
                const u16 hv = (u16)pkh(hh ? kv[4 + e] : kv[8 + e], 0.0f);
                *(u16*)(FT + ad[e]) = hv;
                *(u16*)(FT + ad[e] + 190) = hv;
            }
            k1024 = __builtin_amdgcn_readfirstlane(pkh(kv[8], kv[8]));
        }
        const v4u bd0 = band_frag(-15 + 8 * g4 + l15 - 4 + 1, 3), bd1 = band_frag(-15 + 8 * g4 + l15 - 8 + 2, 5), bd2 = band_frag(-15 + 8 * g4 + l15 - 12 + 1, 3);
        const v4u bd3 = band_frag(-7 + 8 * g4 + l15 - 16 + 2, 5), bd4 = band_frag(-15 + 8 * g4 + l15 - 8 + 3, 7), bd5 = band_frag(-7 + 8 * g4 + l15 - 16 + 4, 9);
        v4u an = band_frag(l15 + 8 * g4 - 18, 2);
#pragma unroll
        for (int k = 0; k < 4; k++) an[k] = ((an[k] >> 10) & 0x00010001u) * 0xffffu & k1024;
        lds_wait_ge(lds, C_U, 4);
        char* const YB = XY;
        auto ldUa = [&](int t) { const int q = 2 * (16 * t + l15) + 1 - g4 + 68; return *(const v4u*)(U + (q + (q >> 2)) * 16); };
        auto ldUb = [&](int t) { const int q = 2 * (16 * t + l15) - g4 + 68; return *(const v4u*)(U + (q + (q >> 2)) * 16); };
        const int e4 = 79 - l15 + 4 * g4, e8 = 127 - l15 + 4 * g4;
        const char* const f4b = FT + (e4 & 1) * 192 + (e4 >> 1) * 4;
        const char* const f8b = FT + (e8 & 1) * 192 + (e8 >> 1) * 4;
        auto ldTf = [&](const char* bp, int ka, int d) {
            const unsigned* pa_ = (const unsigned*)(bp + ka * 384 - 32 * d); const unsigned* pb_ = (const unsigned*)(bp + (ka + 1) * 384 - 32 * d);
            return (v4u){pa_[0], pa_[1], pb_[0], pb_[1]};
        };
        const f4 z4 = {0, 0, 0, 0};
        auto pkW = [&](f4 a, f4 b) { return (v4u){pkh(a[0], a[1]), pkh(a[2], a[3]), pkh(b[0], b[1]), pkh(b[2], b[3])}; };
        f4 F[8];
#pragma unroll
        for (int t = 2; t < 8; t++) F[t] = z4;
        {
            v4u t01[3], t23[3], t45[4];
#pragma unroll
            for (int d = 0; d < 3; d++) { t01[d] = ldTf(f4b, 0, d + 2); t23[d] = ldTf(f4b, 2, d + 2); }
#pragma unroll
            for (int d = 0; d < 4; d++) t45[d] = ldTf(f8b, 4, d + 4);
#pragma unroll
            for (int tp = 0; tp < 6; tp++) {
                const v4u ua = ldUa(tp), ub = ldUb(tp);
                const v4u w01 = pkW(MFMA16(as_h8(ua), as_h8(bd0), z4), MFMA16(as_h8(ua), as_h8(bd1), z4));
                const v4u w23 = pkW(MFMA16(as_h8(ua), as_h8(bd2), z4), MFMA16(as_h8(ub), as_h8(bd3), z4));
#pragma unroll
                for (int d = 2; d <= 4; d++) if (tp + d < 8) {
                    F[tp + d] = MFMA16(as_h8(w01), as_h8(t01[d - 2]), F[tp + d]);
                    F[tp + d] = MFMA16(as_h8(w23), as_h8(t23[d - 2]), F[tp + d]);
                }
                if (tp < 4) {
                    const v4u w45 = pkW(MFMA16(as_h8(ua), as_h8(bd4), z4), MFMA16(as_h8(ub), as_h8(bd5), z4));
#pragma unroll
                    for (int d = 4; d <= 7; d++) if (tp + d < 8) F[tp + d] = MFMA16(as_h8(w45), as_h8(t45[d - 4]), F[tp + d]);
                    F[tp + 4] = MFMA16(as_h8(an), as_h8(ua), F[tp + 4]);
                }
            }
        }
        {
            const int s0 = 4 * l15 + g4;
            *(f4*)(YB + (s0 + (s0 >> 3)) * 16) = z4; *(f4*)(YB + (72 + s0 + (s0 >> 3)) * 16) = z4;
#pragma unroll
            for (int t = 2; t < 8; t++) *(f4*)(YB + (72 * t + s0 + (s0 >> 3)) * 16) = F[t];
        }
        const int rR = 16 * ((l31 >> 2) & 1) + 4 * (l31 >> 3) + (l31 & 3);
        const int tau = rR + 8 * hh + 1;
        const char* const kb = KD + (tau & 1) * KDO + (tau >> 1) * 4;
        const char* const ub0 = U + (5 * l31 - hh + 85) * 16;
        char* const yw = YB + (9 * l31 + 4 * hh) * 16;
        const int ysl = (T + (T >> 3)) * 16;
        unsigned idx = 0;
#pragma unroll
        for (int be = 0; be < 2; be++) {
            f16v acc;
#pragma unroll
            for (int g = 0; g < 4; g++) { const f4 fv = *(const f4*)(yw + be * 288 * 16 + g * 16); acc[4 * g] = fv[0]; acc[4 * g + 1] = fv[1]; acc[4 * g + 2] = fv[2]; acc[4 * g + 3] = fv[3]; }
#pragma unroll
            for (int j = 0; j < 35; j++) {
                const int t = 3 - 2 * j;
                const int fl = (t >= 0) ? (t >> 2) : -((-t + 3) >> 2);
                const v4u ka = ld4dw(kb + 32 * j);
                const v4u ub = *(const v4u*)(ub0 + (be * 160 + t + fl) * 16);
                acc = MFMA32(as_h8(ka), as_h8(ub), acc);
            }
#pragma unroll
            for (int g = 0; g < 4; g++) *(f4*)(yw + be * 288 * 16 + g * 16) = (f4){acc[4 * g] * osc, acc[4 * g + 1] * osc, acc[4 * g + 2] * osc, acc[4 * g + 3] * osc};
            lds_arrive(lds, C_Y0 + be, lane);
            if (be == 0) idx = lds_wait_ge(lds, C_IDX, 1) - 1;
            lds_wait_ge(lds, C_Y0 + be, 4);
            f4 yv[4];
#pragma unroll
            for (int c = 0; c < 4; c++) yv[c] = *(const f4*)(lds + L_XY + c * XYB + be * 288 * 16 + ysl);
#pragma unroll
            for (int r = 0; r < 4; r++) {
                const int row = 1024 * be + 4 * T + r;
                const float o0 = yv[0][r] + base[4 * be + r][0], o1 = yv[1][r] + base[4 * be + r][1], o2 = yv[2][r] + base[4 * be + r][2], o3 = yv[3][r] + base[4 * be + r][3];
                if (row != (int)idx) __builtin_amdgcn_raw_buffer_store_b64((v2u){pkbf(o0, o1), pkbf(o2, o3)}, rout, row * 2048 + cg * 8, 0, 0);
                else {
                    *(f4*)(lds + L_CT + C_ROWV * 4) = (f4){o0, o1, o2, o3};
                    asm volatile("s_waitcnt lgkmcnt(0)" ::: "memory");
                    __hip_atomic_store((unsigned*)(lds + L_CT) + C_ROWF, 1u, __ATOMIC_RELAXED, __HIP_MEMORY_SCOPE_WORKGROUP);
                }
            }
        }
    } else {
        const int wv = wave - 4;
        const bool moe = (b >> 3) < 28;
        const int mo = (b & 7) * 28 + (b >> 3), fb = mo >> 2, kq = mo & 3;
        const v4u cnd = __builtin_amdgcn_raw_buffer_load_b128(rws, WS_CAND + lane * 16, 0, 0);
        if (wv == 0 && lane < 32) ((unsigned*)(lds + L_CT))[lane] = 0;
        asm volatile("s_waitcnt lgkmcnt(0)" ::: "memory");
        __builtin_amdgcn_s_barrier();
        if (!moe) {
            if (wv != 0) return;
        }
        const int k0 = kq * 256 + wv * 64 + (lane >> 3) * 8;
        v4u wgv[8], wuv[8];
        {
            auto rg = mkrs(A.wg, moe ? DM * FF * 2 : 0), ru = mkrs(A.wu, moe ? DM * FF * 2 : 0);
#pragma unroll
            for (int i = 0; i < 8; i++) {
                wgv[i] = __builtin_amdgcn_raw_buffer_load_b128(rg, (k0 + i) * 7168 + fb * 128 + (lane & 7) * 16, 0, AUX_NT);
                wuv[i] = __builtin_amdgcn_raw_buffer_load_b128(ru, (k0 + i) * 7168 + fb * 128 + (lane & 7) * 16, 0, AUX_NT);
            }
        }
        const unsigned cn0 = cnd.x, cn1 = cnd.y, cn2 = cnd.z, cn3 = cnd.w;
        const unsigned best = __builtin_amdgcn_readfirstlane(wave_umax(max(max(cn0, cn1), max(cn2, cn3))));
        const unsigned idx = 2047 - (best & 0xffffu);
        const unsigned key = best >> 16;
        const float rsel = bflo((key & 0x8000u) ? (key & 0x7fffu) : (~key & 0xffffu));
        if (wv == 0 && lane == 0) __hip_atomic_store((unsigned*)(lds + L_CT) + C_IDX, idx + 1, __ATOMIC_RELAXED, __HIP_MEMORY_SCOPE_WORKGROUP);
        bool fin = (wv == 0);
        if (moe) {
            auto rx = mkrs(A.x, LSEQ * DM * 2);
            const v4u xs = __builtin_amdgcn_raw_buffer_load_b128(rx, idx * 2048 + k0 * 2, 0, 0);
            const unsigned ms = __builtin_amdgcn_raw_buffer_load_b32(rws, WS_ST + idx * 4, 0, 0);
            v2u wdv[16];
            auto rd = mkrs(A.wd, DM * FF * 2);
#pragma unroll
            for (int i = 0; i < 16; i++) wdv[i] = __builtin_amdgcn_raw_buffer_load_b64(rd, (fb * 64 + wv * 16 + i) * 2048 + kq * 512 + lane * 8, 0, AUX_NT);
            const float m = bflo(ms), rinv = __builtin_amdgcn_rcpf(bfhi(ms));
            float acc[16];
#pragma unroll
            for (int c = 0; c < 16; c++) acc[c] = 0;
#pragma unroll
            for (int i2 = 0; i2 < 4; i2++) {
                const unsigned dp = pkbf(bflo(xs[i2]) - m, bfhi(xs[i2]) - m);
                const unsigned np = pkbf(bflo(dp) * rinv, bfhi(dp) * rinv);
#pragma unroll
                for (int s = 0; s < 2; s++) {
                    const float xk = s ? bfhi(np) : bflo(np);
                    const int i = 2 * i2 + s;
#pragma unroll
                    for (int c = 0; c < 4; c++) {
                        acc[2 * c] = fmaf(xk, bflo(wgv[i][c]), acc[2 * c]); acc[2 * c + 1] = fmaf(xk, bfhi(wgv[i][c]), acc[2 * c + 1]);
                        acc[8 + 2 * c] = fmaf(xk, bflo(wuv[i][c]), acc[8 + 2 * c]); acc[8 + 2 * c + 1] = fmaf(xk, bfhi(wuv[i][c]), acc[8 + 2 * c + 1]);
                    }
                }
            }
            float r8[8], r4[4], r2[2];
#pragma unroll
            for (int c = 0; c < 8; c++) { f2 t = swap32(acc[c], acc[8 + c]); r8[c] = t.x + t.y; }
#pragma unroll
            for (int c = 0; c < 4; c++) { f2 t = swap16(r8[c], r8[4 + c]); r4[c] = t.x + t.y; }
            const bool b3 = lane & 8;
#pragma unroll
            for (int c = 0; c < 2; c++) { const float keep = b3 ? r4[2 + c] : r4[c], give = b3 ? r4[c] : r4[2 + c]; r2[c] = keep + dpp_ror8(give); }
            const int vi = (lane >> 5) * 64 + 8 * (lane & 7) + 4 * ((lane >> 4) & 1) + 2 * ((lane >> 3) & 1);
            *(f2*)(lds + L_GUR + wv * 512 + vi * 4) = (f2){r2[0], r2[1]};
            if (lds_arrive_rtn(lds, C_GU, lane) == 3) {
                const f2 s0 = *(const f2*)(lds + L_GUR + lane * 8), s1 = *(const f2*)(lds + L_GUR + 512 + lane * 8), s2 = *(const f2*)(lds + L_GUR + 1024 + lane * 8), s3 = *(const f2*)(lds + L_GUR + 1536 + lane * 8);
                const f2 s = (s0 + s1) + (s2 + s3);
                __builtin_amdgcn_raw_buffer_store_b64((v2u){__float_as_uint(s.x) | 1u, __float_as_uint(s.y) | 1u}, rws, WS_GU + mo * 512 + lane * 8, 0, AUX_SC1);
            }
            unsigned gq, uq;
            _Pragma("nounroll") for (unsigned s = 0; s < SPIN_CAP; s++) {
                gq = __builtin_amdgcn_raw_buffer_load_b32(rws, WS_GU + (fb * 4 + (lane >> 4)) * 512 + (16 * wv + (lane & 15)) * 4, 0, AUX_SC1);
                uq = __builtin_amdgcn_raw_buffer_load_b32(rws, WS_GU + (fb * 4 + (lane >> 4)) * 512 + 256 + (16 * wv + (lane & 15)) * 4, 0, AUX_SC1);
                if (__all((gq & uq & 1u) != 0)) break;
                __builtin_amdgcn_s_sleep(2);
            }
            const float gt = rbf(sum32(sum16(__uint_as_float(gq)))), ut = rbf(sum32(sum16(__uint_as_float(uq))));
            const float hv = rbf(rbf(gt / (1.0f + expf(-gt))) * ut);
            float p4[4] = {0, 0, 0, 0};
#pragma unroll
            for (int i = 0; i < 16; i++) {
                const float hs = __uint_as_float(__builtin_amdgcn_readlane(__float_as_uint(hv), i));
                p4[0] = fmaf(hs, bflo(wdv[i].x), p4[0]); p4[1] = fmaf(hs, bfhi(wdv[i].x), p4[1]);
                p4[2] = fmaf(hs, bflo(wdv[i].y), p4[2]); p4[3] = fmaf(hs, bfhi(wdv[i].y), p4[3]);
            }
            *(f4*)(lds + L_PR + wv * 1024 + lane * 16) = (f4){p4[0], p4[1], p4[2], p4[3]};
            fin = false;
            if (lds_arrive_rtn(lds, C_P, lane) == 3) {
                const f4 s0 = *(const f4*)(lds + L_PR + lane * 16), s1 = *(const f4*)(lds + L_PR + 1024 + lane * 16), s2 = *(const f4*)(lds + L_PR + 2048 + lane * 16), s3 = *(const f4*)(lds + L_PR + 3072 + lane * 16);
                const f4 s = (s0 + s1) + (s2 + s3);
                __builtin_amdgcn_raw_buffer_store_b128((v4u){__float_as_uint(s[0]) | 1u, __float_as_uint(s[1]) | 1u, __float_as_uint(s[2]) | 1u, __float_as_uint(s[3]) | 1u},
                                                       rws, WS_P + fb * 4096 + kq * 1024 + lane * 16, 0, AUX_SC1);
                fin = true;
            }
        }
        if (fin) {
            v4u pv;
            const int pr = lane < 56 ? lane : 55;
            _Pragma("nounroll") for (unsigned s = 0; s < SPIN_CAP; s++) {
                pv = __builtin_amdgcn_raw_buffer_load_b128(rws, WS_P + pr * 4096 + cg * 16, 0, AUX_SC1);
                if (__all((pv.x & pv.y & pv.z & pv.w & 1u) != 0)) break;
                __builtin_amdgcn_s_sleep(2);
            }
            float y4[4];
#pragma unroll
            for (int c = 0; c < 4; c++) y4[c] = wave_sum(lane < 56 ? __uint_as_float(pv[c]) : 0.0f);
            const float rs = rbf(1.0f / (1.0f + expf(-rsel)));
            lds_wait_ge(lds, C_ROWF, 1);
            const f4 rv = *(const f4*)(lds + L_CT + C_ROWV * 4);
            float o[4];
#pragma unroll
            for (int c = 0; c < 4; c++) o[c] = rv[c] + rbf(rbf(y4[c]) * rs);
            if (lane == 0) __builtin_amdgcn_raw_buffer_store_b64((v2u){pkbf(o[0], o[1]), pkbf(o[2], o[3])}, rout, idx * 2048 + cg * 8, 0, 0);
        }
    }
}

extern "C" void kernel_launch(void* const* d_in, const int* in_sizes, int n_in,
                              void* d_out, int out_size, void* d_ws, size_t ws_size,
                              hipStream_t stream) {
    K2Args a;
    a.x = (const u16*)d_in[0]; a.lar = (const u16*)d_in[1]; a.aim = (const u16*)d_in[2]; a.cre = (const u16*)d_in[3]; a.cim = (const u16*)d_in[4];
    a.ldt = (const u16*)d_in[5]; a.dsk = (const u16*)d_in[6]; a.wg = (const u16*)d_in[9]; a.wu = (const u16*)d_in[10]; a.wd = (const u16*)d_in[11];
    a.out = (u16*)d_out; a.ws = (char*)d_ws;
    k1_ln<<<256, 256, 0, stream>>>(a.x, (const u16*)d_in[8], a.ws);
    k2_main<<<256, 512, 0, stream>>>(a);
}
```

```cpp
#include <hip/hip_runtime.h>
#include <stdint.h>

typedef unsigned short u16;
typedef unsigned v4u __attribute__((ext_vector_type(4)));
typedef unsigned v2u __attribute__((ext_vector_type(2)));
typedef float f4 __attribute__((ext_vector_type(4)));
typedef float f2 __attribute__((ext_vector_type(2)));
typedef float f16v __attribute__((ext_vector_type(16)));
typedef _Float16 h8 __attribute__((ext_vector_type(8)));
typedef _Float16 h2 __attribute__((ext_vector_type(2)));
typedef __bf16 b2 __attribute__((ext_vector_type(2)));
#define DI __device__ __forceinline__
#define AUX_NT 2
#define AUX_SC1 16

constexpr int LSEQ = 2048, DM = 1024, FF = 3584;
constexpr unsigned WS_ST = 0;
constexpr unsigned WS_CAND = 8192;
constexpr unsigned WS_GU = 16384;
constexpr unsigned WS_P = WS_GU + 224 * 512;
constexpr unsigned WS_HOP_BYTES = 224 * 512 + 56 * 4096;
constexpr unsigned WS_XT = 1u << 20;
constexpr unsigned WS_BYTES = WS_XT + (4u << 20);
constexpr unsigned SPIN_CAP = 1u << 18;

DI float bflo(unsigned v) { return __uint_as_float(v << 16); }
DI float bfhi(unsigned v) { return __uint_as_float(v & 0xffff0000u); }
DI unsigned pkbf(float a, float b) { b2 v = {(__bf16)a, (__bf16)b}; return __builtin_bit_cast(unsigned, v); }
DI float rbf(float a) { return bflo(pkbf(a, a)); }
DI unsigned pkh(float a, float b) { h2 v = {(_Float16)a, (_Float16)b}; return __builtin_bit_cast(unsigned, v); }
DI float dpp_ror4(float x) { return __int_as_float(__builtin_amdgcn_update_dpp(0, __float_as_int(x), 0x124, 0xf, 0xf, false)); }
DI float dpp_ror8(float x) { return __int_as_float(__builtin_amdgcn_update_dpp(0, __float_as_int(x), 0x128, 0xf, 0xf, false)); }
DI float dpp_ror1(float x) { return __int_as_float(__builtin_amdgcn_update_dpp(0, __float_as_int(x), 0x121, 0xf, 0xf, false)); }
DI float dpp_ror2(float x) { return __int_as_float(__builtin_amdgcn_update_dpp(0, __float_as_int(x), 0x122, 0xf, 0xf, false)); }
DI f2 swap16(float a, float b) { auto r = __builtin_amdgcn_permlane16_swap(__float_as_uint(a), __float_as_uint(b), false, false); return (f2){__uint_as_float(r[0]), __uint_as_float(r[1])}; }
DI f2 swap32(float a, float b) { auto r = __builtin_amdgcn_permlane32_swap(__float_as_uint(a), __float_as_uint(b), false, false); return (f2){__uint_as_float(r[0]), __uint_as_float(r[1])}; }
DI float sum16(float a) { f2 r = swap16(a, a); return r.x + r.y; }
DI float sum32(float a) { f2 r = swap32(a, a); return r.x + r.y; }
DI float wave_sum(float v) { v += dpp_ror1(v); v += dpp_ror2(v); v += dpp_ror4(v); v += dpp_ror8(v); v = sum16(v); return sum32(v); }
DI float wave_max(float v) {
    v = fmaxf(v, dpp_ror1(v)); v = fmaxf(v, dpp_ror2(v)); v = fmaxf(v, dpp_ror4(v)); v = fmaxf(v, dpp_ror8(v));
    f2 r = swap16(v, v); v = fmaxf(r.x, r.y); r = swap32(v, v); return fmaxf(r.x, r.y);
}
DI unsigned wave_umax(unsigned v) {
    v = max(v, (unsigned)__builtin_amdgcn_update_dpp(0, (int)v, 0x121, 0xf, 0xf, false));
    v = max(v, (unsigned)__builtin_amdgcn_update_dpp(0, (int)v, 0x122, 0xf, 0xf, false));
    v = max(v, (unsigned)__builtin_amdgcn_update_dpp(0, (int)v, 0x124, 0xf, 0xf, false));
    v = max(v, (unsigned)__builtin_amdgcn_update_dpp(0, (int)v, 0x128, 0xf, 0xf, false));
    auto r = __builtin_amdgcn_permlane16_swap(v, v, false, false); v = max(r[0], r[1]);
    r = __builtin_amdgcn_permlane32_swap(v, v, false, false); return max(r[0], r[1]);
}
DI __amdgpu_buffer_rsrc_t mkrs(const void* p, unsigned bytes) { return __builtin_amdgcn_make_buffer_rsrc((void*)p, 0, bytes, 0x00020000); }

#define SDECL
#define STAMP(i)
#define SFLUSH(wsp, kern, nw)
__global__ void __launch_bounds__(256) k1_ln(const u16* __restrict__ x, const u16* __restrict__ wr, char* __restrict__ ws) {
    __shared__ f2 sst[4][8];
    __shared__ float srr[4][8];
    const int b = blockIdx.x, tid = threadIdx.x, lane = tid & 63, wave = tid >> 6;
    const int g = b >> 2, j = b & 3, a = lane >> 2, i = lane & 3;
    const int R0 = 1024 * (j >> 1) + 16 * g + 4 * i + 2 * (j & 1);
    SDECL;
    auto rx = mkrs(x, LSEQ * DM * 2);
    auto rw = mkrs(wr, DM * 2);
    auto rws = mkrs(ws, WS_BYTES);
    v4u v[2][2], w[2];
#pragma unroll
    for (int rr = 0; rr < 2; rr++)
#pragma unroll
        for (int kk = 0; kk < 2; kk++) v[rr][kk] = __builtin_amdgcn_raw_buffer_load_b128(rx, (R0 + rr) * 2048 + (a + 16 * (2 * wave + kk)) * 16, 0, AUX_NT);
#pragma unroll
    for (int kk = 0; kk < 2; kk++) w[kk] = __builtin_amdgcn_raw_buffer_load_b128(rw, (a + 16 * (2 * wave + kk)) * 16, 0, 0);
    if (tid < 84) __builtin_amdgcn_raw_buffer_store_b128((v4u){0, 0, 0, 0}, rws, WS_GU + b * 1344 + tid * 16, 0, 0);
#pragma unroll
    for (int kk = 0; kk < 2; kk++)
#pragma unroll
        for (int hh = 0; hh < 2; hh++) {
            const int cg = 2 * (a + 16 * (2 * wave + kk)) + hh;
            v4u pc = {v[0][kk][2 * hh], v[0][kk][2 * hh + 1], v[1][kk][2 * hh], v[1][kk][2 * hh + 1]};
            __builtin_amdgcn_raw_buffer_store_b128(pc, rws, WS_XT + cg * 16384 + j * 4096 + (4 * g + i) * 16, 0, 0);
        }
    STAMP(1);
    float f[2][16], mean[2], m2[2];
#pragma unroll
    for (int rr = 0; rr < 2; rr++) {
#pragma unroll
        for (int kk = 0; kk < 2; kk++)
#pragma unroll
            for (int e = 0; e < 4; e++) { f[rr][kk * 8 + 2 * e] = bflo(v[rr][kk][e]); f[rr][kk * 8 + 2 * e + 1] = bfhi(v[rr][kk][e]); }
        float s = 0;
#pragma unroll
        for (int e = 0; e < 16; e++) s += f[rr][e];
        s *= (1.0f / 16);
        float q = 0;
#pragma unroll
        for (int e = 0; e < 16; e++) { float d = f[rr][e] - s; q += d * d; }
        mean[rr] = s; m2[rr] = q;
    }
#pragma unroll
    for (int rr = 0; rr < 2; rr++) {
        float m = mean[rr], q = m2[rr], mo, qo, d;
        mo = dpp_ror8(m); qo = dpp_ror8(q); d = m - mo; q = q + qo + d * d * 8.0f; m = 0.5f * (m + mo);
        mo = dpp_ror4(m); qo = dpp_ror4(q); d = m - mo; q = q + qo + d * d * 16.0f; m = 0.5f * (m + mo);
        f2 rm = swap16(m, m), rq = swap16(q, q); d = rm.x - rm.y; q = rq.x + rq.y + d * d * 32.0f; m = 0.5f * (rm.x + rm.y);
        rm = swap32(m, m); rq = swap32(q, q); d = rm.x - rm.y; q = rq.x + rq.y + d * d * 64.0f; m = 0.5f * (rm.x + rm.y);
        if (a == 0) sst[wave][2 * i + rr] = (f2){m, q};
    }
    STAMP(2);
    __syncthreads();
    STAMP(3);
    float racc[2]; unsigned msb[2];
#pragma unroll
    for (int rr = 0; rr < 2; rr++) {
        f2 s0 = sst[0][2 * i + rr], s1 = sst[1][2 * i + rr], s2 = sst[2][2 * i + rr], s3 = sst[3][2 * i + rr];
        float d01 = s0.x - s1.x, d23 = s2.x - s3.x;
        float ma = 0.5f * (s0.x + s1.x), mb = 0.5f * (s2.x + s3.x);
        float qa = s0.y + s1.y + d01 * d01 * 128.0f, qb = s2.y + s3.y + d23 * d23 * 128.0f;
        float dd = ma - mb;
        float mt = 0.5f * (ma + mb), qt = qa + qb + dd * dd * 256.0f;
        float mbf = rbf(mt), vbf = rbf(qt * (1.0f / 1024));
        float sd = rbf(sqrtf(rbf(vbf + 1e-5f)));
        float rinv = 1.0f / sd;
        msb[rr] = (__float_as_uint(mbf) >> 16) | (__float_as_uint(sd) & 0xffff0000u);
        float acc = 0;
#pragma unroll
        for (int kk = 0; kk < 2; kk++)
#pragma unroll
            for (int e = 0; e < 4; e++) {
                unsigned dp = pkbf(f[rr][kk * 8 + 2 * e] - mbf, f[rr][kk * 8 + 2 * e + 1] - mbf);
                unsigned np = pkbf(bflo(dp) * rinv, bfhi(dp) * rinv);
                acc += bflo(np) * bflo(w[kk][e]); acc += bfhi(np) * bfhi(w[kk][e]);
            }
        acc += dpp_ror8(acc); acc += dpp_ror4(acc); acc = sum16(acc); acc = sum32(acc);
        racc[rr] = acc;
        if (a == 0) srr[wave][2 * i + rr] = acc;
    }
    if (wave == 0 && a == 0) __builtin_amdgcn_raw_buffer_store_b64((v2u){msb[0], msb[1]}, rws, WS_ST + R0 * 4, 0, 0);
    __syncthreads();
    if (tid < 8) {
        float r = (srr[0][tid] + srr[1][tid]) + (srr[2][tid] + srr[3][tid]);
        unsigned rb = pkbf(r, r) & 0xffffu;
        unsigned key = (rb & 0x8000u) ? (~rb & 0xffffu) : (rb | 0x8000u);
        const int row = 1024 * (j >> 1) + 16 * g + 4 * (tid >> 1) + 2 * (j & 1) + (tid & 1);
        unsigned k2 = (key << 16) | (unsigned)(2047 - row);
        k2 = max(k2, (unsigned)__builtin_amdgcn_update_dpp(0, (int)k2, 0x111, 0xf, 0xf, false));
        k2 = max(k2, (unsigned)__builtin_amdgcn_update_dpp(0, (int)k2, 0x112, 0xf, 0xf, false));
        k2 = max(k2, (unsigned)__builtin_amdgcn_update_dpp(0, (int)k2, 0x114, 0xf, 0xf, false));
        if (tid == 7) __builtin_amdgcn_raw_buffer_store_b32(k2, rws, WS_CAND + b * 4, 0, 0);
    }
    STAMP(4);
    STAMP(5);
    SFLUSH(ws, 0, 8);
}

constexpr int UB = 6464;
constexpr int L_U = 0;
constexpr int KDB = 2432, KDO = 1216;
constexpr int L_KD = L_U + 4 * UB;
constexpr int FTB = 2304;
constexpr int L_FT = L_KD + 4 * KDB;
constexpr int XYB = 53 * 512;
constexpr int L_XY = L_FT + 4 * FTB;
constexpr int L_MO = L_XY + 4 * XYB;
constexpr int L_GUR = L_MO, L_PR = L_MO + 2048;
constexpr int L_CT = L_PR + 4096;
constexpr int L_TOT = L_CT + 128;
enum { C_U = 0, C_Y0 = 1, C_Y1 = 2, C_GU = 3, C_P = 4, C_IDX = 5, C_ROWF = 6, C_DUMMY = 7, C_ROWV = 8, C_OSC = 12 };

struct K2Args {
    const u16* x; const u16* lar; const u16* aim; const u16* cre; const u16* cim; const u16* ldt; const u16* dsk;
    const u16* wg; const u16* wu; const u16* wd; u16* out; char* ws;
};

DI void lds_arrive(char* lds, int word, int lane) {
    asm volatile("s_waitcnt lgkmcnt(0)" ::: "memory");
    if (lane == 0) __hip_atomic_fetch_add((unsigned*)(lds + L_CT) + word, 1u, __ATOMIC_RELAXED, __HIP_MEMORY_SCOPE_WORKGROUP);
}
DI unsigned lds_arrive_rtn(char* lds, int word, int lane) {
    asm volatile("s_waitcnt lgkmcnt(0)" ::: "memory");
    unsigned r = 0;
    if (lane == 0) r = __hip_atomic_fetch_add((unsigned*)(lds + L_CT) + word, 1u, __ATOMIC_RELAXED, __HIP_MEMORY_SCOPE_WORKGROUP);
    r = __builtin_amdgcn_readfirstlane(r);
    asm volatile("" ::: "memory");
    return r;
}
DI unsigned lds_wait_ge(char* lds, int word, unsigned want) {
    unsigned v;
    _Pragma("nounroll") for (unsigned s = 0; s < (1u << 24); s++) {
        v = __builtin_amdgcn_readfirstlane(__hip_atomic_load((unsigned*)(lds + L_CT) + word, __ATOMIC_RELAXED, __HIP_MEMORY_SCOPE_WORKGROUP));
        if (v >= want) break;
        __builtin_amdgcn_s_sleep(1);
    }
    asm volatile("" ::: "memory");
    return v;
}
struct cf { float x, y; };
DI cf cmul(cf a, cf b) { return {fmaf(a.x, b.x, -(a.y * b.y)), fmaf(a.x, b.y, a.y * b.x)}; }
DI cf cexp_p(float ar2, float air, float p) {
    float m = __builtin_amdgcn_exp2f(ar2 * p);
    float t = __builtin_amdgcn_fractf(air * p);
    return {m * __builtin_amdgcn_cosf(t), m * __builtin_amdgcn_sinf(t)};
}
DI h8 as_h8(v4u v) { return __builtin_bit_cast(h8, v); }
DI v4u ld4dw(const char* p) { const unsigned* q = (const unsigned*)p; return (v4u){q[0], q[1], q[2], q[3]}; }
DI v4u band_frag(int v0, int width) {
    unsigned long long m = ((1ull << width) - 1) << 32;
    unsigned m8 = (unsigned)(m >> (v0 + 32)) & 0xffu;
    v4u r;
#pragma unroll
    for (int k = 0; k < 4; k++) { unsigned t = m8 >> (2 * k); r[k] = ((t & 1u) | ((t & 2u) << 15)) * 0x3C00u; }
    return r;
}
#define MFMA32(a, b, c) __builtin_amdgcn_mfma_f32_32x32x16_f16((a), (b), (c), 0, 0, 0)
#define MFMA16(a, b, c) __builtin_amdgcn_mfma_f32_16x16x32_f16((a), (b), (c), 0, 0, 0)

__global__ void __launch_bounds__(512, 2) k2_main(K2Args A) {
    __shared__ __attribute__((aligned(16))) char lds[L_TOT];
    const int b = blockIdx.x, tid = threadIdx.x, lane = tid & 63;
    const int wave = __builtin_amdgcn_readfirstlane(tid >> 6);
    const int cg = (b & 7) * 32 + (b >> 3);
    SDECL;
    auto rws = mkrs(A.ws, WS_BYTES);
    auto rout = mkrs(A.out, LSEQ * DM * 2);
    if (wave < 4) {
        const int T = tid, ch = cg * 4 + wave;
        const unsigned pl = *(const unsigned*)(A.lar + ch * 128 + 2 * lane), pa = *(const unsigned*)(A.aim + ch * 128 + 2 * lane);
        const unsigned pcr = *(const unsigned*)(A.cre + ch * 128 + 2 * lane), pci = *(const unsigned*)(A.cim + ch * 128 + 2 * lane);
        const float ldt = bflo(A.ldt[ch]);
        v4u xt[4], st[2];
#pragma unroll
        for (int j = 0; j < 4; j++) xt[j] = __builtin_amdgcn_raw_buffer_load_b128(rws, WS_XT + cg * 16384 + j * 4096 + T * 16, 0, 0);
        st[0] = __builtin_amdgcn_raw_buffer_load_b128(rws, WS_ST + T * 16, 0, 0);
        st[1] = __builtin_amdgcn_raw_buffer_load_b128(rws, WS_ST + 4096 + T * 16, 0, 0);
        const v2u dsk2 = *(const v2u*)(A.dsk + cg * 4);
        char* const U = lds + L_U + wave * UB;
        char* const KD = lds + L_KD + wave * KDB;
        char* const FT = lds + L_FT + wave * FTB;
        char* const XY = lds + L_XY + wave * XYB;
        {
            const v4u z = {0, 0, 0, 0};
            *(v4u*)(U + lane * 16) = z; if (lane < 21) *(v4u*)(U + 1024 + lane * 16) = z;
            *(v4u*)(FT + lane * 16) = z; *(v4u*)(FT + 1024 + lane * 16) = z; if (lane < 16) *(v4u*)(FT + 2048 + lane * 16) = z;
            if (lane < 24) { int o = lane < 12 ? lane : lane - 12; int off = (o < 4 ? o * 16 : 1088 + (o - 4) * 16) + (lane < 12 ? 0 : KDO); *(v4u*)(KD + off) = z; }
        }
        __builtin_amdgcn_s_barrier();
        STAMP(1);
        const int l31 = lane & 31, hh = lane >> 5, l15 = lane & 15, g4 = lane >> 4;
        float osc;
        {
            const float dt = rbf(expf(ldt));
            const float LOG2E = 1.4426950408889634f, INV2PI = 0.15915494309189535f;
            float ar2[2], air[2]; cf C[2];
            ar2[0] = (-rbf(expf(bflo(pl))) * dt) * LOG2E; ar2[1] = (-rbf(expf(bfhi(pl))) * dt) * LOG2E;
            air[0] = (bflo(pa) * dt) * INV2PI; air[1] = (bfhi(pa) * dt) * INV2PI;
            float mx = fmaxf(fabsf(bflo(pcr)) + fabsf(bflo(pci)), fabsf(bfhi(pcr)) + fabsf(bfhi(pci))) * (2.0f * dt);
            mx = wave_max(mx);
            int E = (__float_as_uint(mx) >> 23) & 0xff; E = min(max(E, 20), 240);
            const float sc = __uint_as_float((unsigned)(267 - E) << 23);
            osc = __uint_as_float((unsigned)(E - 14) << 23);
            const float cs = 2.0f * sc;
            C[0] = {bflo(pcr) * dt * cs, bflo(pci) * dt * cs}; C[1] = {bfhi(pcr) * dt * cs, bfhi(pci) * dt * cs};
            STAMP(2);
            const int sl = lane >> 1, hb = (lane & 1) * 8;
            auto putX = [&](int row, cf a0, cf a1) { *(v2u*)(XY + row * 512 + ((sl ^ (row & 15)) << 4) + hb) = (v2u){pkh(a0.x, -a0.y), pkh(a1.x, -a1.y)}; };
            auto putY = [&](int row, cf a0, cf a1) { *(v2u*)(XY + (21 + row) * 512 + ((sl ^ (row & 15)) << 4) + hb) = (v2u){pkh(a0.x, a0.y), pkh(a1.x, a1.y)}; };
            cf z[2], w[2], zb[2], z2[2];
#pragma unroll
            for (int m = 0; m < 2; m++) { z[m] = cexp_p(ar2[m], air[m], 1.0f); w[m] = cexp_p(ar2[m], air[m], 8.0f); zb[m] = cexp_p(ar2[m], air[m], 256.0f); z2[m] = cexp_p(ar2[m], air[m], 2.0f); }
            {
                cf y[4][2];
#pragma unroll
                for (int m = 0; m < 2; m++) { y[0][m] = {1.0f, 0.0f}; y[1][m] = cexp_p(ar2[m], air[m], 64.0f); y[2][m] = cexp_p(ar2[m], air[m], 128.0f); y[3][m] = cexp_p(ar2[m], air[m], 192.0f); }
#pragma unroll
                for (int s = 0; s < 8; s++) {
#pragma unroll
                    for (int c = 0; c < 4; c++) {
                        putY(8 * c + s, y[c][0], y[c][1]);
                        if (s < 7) { y[c][0] = cmul(y[c][0], w[0]); y[c][1] = cmul(y[c][1], w[1]); }
                    }
                }
            }
            {
                cf t[2] = {C[0], C[1]};
#pragma unroll
                for (int e = 0; e < 8; e++) { putX(e, t[0], t[1]); if (e < 7) { t[0] = cmul(t[0], z[0]); t[1] = cmul(t[1], z[1]); } }
                cf p256[2] = {cmul(C[0], zb[0]), cmul(C[1], zb[1])};
                cf p258[2] = {cmul(p256[0], z2[0]), cmul(p256[1], z2[1])};
                cf p260[2] = {cmul(p258[0], z2[0]), cmul(p258[1], z2[1])};
                cf p262[2] = {cmul(p260[0], z2[0]), cmul(p260[1], z2[1])};
                putX(8, p256[0], p256[1]); putX(9, p258[0], p258[1]); putX(10, p260[0], p260[1]); putX(11, p262[0], p262[1]);
                cf p512[2] = {cmul(p256[0], zb[0]), cmul(p256[1], zb[1])};
                cf p516[2] = {cmul(p260[0], zb[0]), cmul(p260[1], zb[1])};
                cf p768[2] = {cmul(p512[0], zb[0]), cmul(p512[1], zb[1])};
                cf p772[2] = {cmul(p516[0], zb[0]), cmul(p516[1], zb[1])};
                putX(12, p512[0], p512[1]); putX(13, p516[0], p516[1]); putX(14, p768[0], p768[1]); putX(15, p772[0], p772[1]);
                t[0] = p768[0]; t[1] = p768[1];
#pragma unroll
                for (int e = 0; e < 5; e++) { t[0] = cmul(t[0], zb[0]); t[1] = cmul(t[1], zb[1]); putX(16 + e, t[0], t[1]); }
            }
        }
        STAMP(3);
        f16v kacc;
#pragma unroll
        for (int e = 0; e < 16; e++) kacc[e] = 0.0f;
        {
            const int lp = hh ^ (l31 & 15);
#pragma unroll
            for (int t = 0; t < 16; t++) {
                const int so = ((lp ^ ((2 * t) & 15)) + ((2 * t) & 16)) << 4;
                v4u xa = *(const v4u*)(XY + l31 * 512 + so);
                v4u yb = *(const v4u*)(XY + (21 + l31) * 512 + so);
                kacc = MFMA32(as_h8(xa), as_h8(yb), kacc);
            }
        }
        STAMP(4);
        float base[8][4];
        {
            const float d1[4] = {(1.0f + bflo(dsk2.x)) * (1.0f / 128), (1.0f + bfhi(dsk2.x)) * (1.0f / 128), (1.0f + bflo(dsk2.y)) * (1.0f / 128), (1.0f + bfhi(dsk2.y)) * (1.0f / 128)};
            float un[8][4];
#pragma unroll
            for (int r = 0; r < 8; r++) {
                const unsigned ms = st[r >> 2][r & 3];
                const float m = bflo(ms), rinv = __builtin_amdgcn_rcpf(bfhi(ms)) * 128.0f;
#pragma unroll
                for (int p = 0; p < 2; p++) {
                    const unsigned xv = xt[r >> 1][(r & 1) * 2 + p];
                    const float x0 = bflo(xv), x1 = bfhi(xv);
                    const unsigned dp = pkbf(x0 - m, x1 - m);
                    const unsigned np = pkbf(bflo(dp) * rinv, bfhi(dp) * rinv);
                    un[r][2 * p] = bflo(np); un[r][2 * p + 1] = bfhi(np);
                    base[r][2 * p] = fmaf(un[r][2 * p], d1[2 * p], x0); base[r][2 * p + 1] = fmaf(un[r][2 * p + 1], d1[2 * p + 1], x1);
                }
            }
#pragma unroll
            for (int hf = 0; hf < 2; hf++) {
                const int qp = (T >> 1) + 128 * hf + 68;
                const int off = (qp + (qp >> 2)) * 16 + ((T & 1) ? 0 : 8);
#pragma unroll
                for (int c = 0; c < 4; c++)
                    *(v2u*)(lds + L_U + c * UB + off) = (v2u){pkh(un[4 * hf + 3][c], un[4 * hf + 2][c]), pkh(un[4 * hf + 1][c], un[4 * hf][c])};
            }
        }
        lds_arrive(lds, C_U, lane);
        STAMP(5);
        unsigned k1024;
        {
            float kv[12];
#pragma unroll
            for (int e = 0; e < 12; e++) kv[e] = kacc[e] * (1.0f / 64);
            const unsigned p01 = pkh(kv[0], kv[1]), p23 = pkh(kv[2], kv[3]), p12 = pkh(kv[1], kv[2]);
            const int o1 = 64 + 16 * l31 + 8 * hh;
            *(v2u*)(KD + o1) = (v2u){p01, p23};
            *(u16*)(KD + KDO + o1 - 2) = (u16)p01;
            *(unsigned*)(KD + KDO + o1) = p12;
            *(u16*)(KD + KDO + o1 + 4) = (u16)(p23 >> 16);
            const float vn = __shfl_down(kv[4], 1);
            if (hh == 0) {
                *(v4u*)(KD + 576 + 16 * l31) = (v4u){pkh(kv[4], kv[4]), pkh(kv[5], kv[6]), pkh(kv[6], kv[6]), pkh(kv[7], vn)};
                *(v4u*)(KD + KDO + 576 + 16 * l31) = (v4u){pkh(kv[4], kv[5]), pkh(kv[6], kv[6]), pkh(kv[6], kv[7]), pkh(vn, vn)};
            }
            if (lane == 0) *(u16*)(KD + KDO + 574) = (u16)pkh(kv[4], kv[4]);
            if (lane == 32) {
                *(v2u*)(KD + 1088) = (v2u){pkh(kv[4], kv[4]), pkh(kv[4], 0.0f)};
                *(unsigned*)(KD + KDO + 1088) = pkh(kv[4], kv[4]);
                *(u16*)(FT + 5 * 384) = (u16)pkh(kv[8], kv[8]);
            }
            const int bo = l31 & 1;
            int a0, a1, st2;
            if (hh) { a0 = (bo ? 1 : 3) * 384 + 2 * (79 - (31 + ((l31 + 1) >> 1))); a1 = (bo ? 2 : 0) * 384 + 2 * (79 - (32 + (l31 >> 1))); st2 = 32; }
            else { a0 = (bo ? 4 : 5) * 384 + 2 * (127 - (63 + ((l31 + 1) >> 1))); a1 = a0 - 32; st2 = 64; }
            int ad[4] = {a0, a1, a0 - st2, a1 - st2};
            if (lane == 0) ad[0] = 3 * 384 + 2 * 16;
            if (lane == 32) ad[0] = 3 * 384 + 2 * 70;
#pragma unroll
            for (int e = 0; e < 4; e++) {
                const u16 hv = (u16)pkh(hh ? kv[4 + e] : kv[8 + e], 0.0f);
                *(u16*)(FT + ad[e]) = hv;
                *(u16*)(FT + ad[e] + 190) = hv;
            }
            k1024 = __builtin_amdgcn_readfirstlane(pkh(kv[8], kv[8]));
        }
        const v4u bd0 = band_frag(-15 + 8 * g4 + l15 - 4 + 1, 3), bd1 = band_frag(-15 + 8 * g4 + l15 - 8 + 2, 5), bd2 = band_frag(-15 + 8 * g4 + l15 - 12 + 1, 3);
        const v4u bd3 = band_frag(-7 + 8 * g4 + l15 - 16 + 2, 5), bd4 = band_frag(-15 + 8 * g4 + l15 - 8 + 3, 7), bd5 = band_frag(-7 + 8 * g4 + l15 - 16 + 4, 9);
        v4u an = band_frag(l15 + 8 * g4 - 18, 2);
#pragma unroll
        for (int k = 0; k < 4; k++) an[k] = ((an[k] >> 10) & 0x00010001u) * 0xffffu & k1024;
        STAMP(6);
        lds_wait_ge(lds, C_U, 4);
        STAMP(7);
        char* const YB = XY;
        auto ldUa = [&](int t) { const int q = 2 * (16 * t + l15) + 1 - g4 + 68; return *(const v4u*)(U + (q + (q >> 2)) * 16); };
        auto ldUb = [&](int t) { const int q = 2 * (16 * t + l15) - g4 + 68; return *(const v4u*)(U + (q + (q >> 2)) * 16); };
        const int e4 = 79 - l15 + 4 * g4, e8 = 127 - l15 + 4 * g4;
        const char* const f4b = FT + (e4 & 1) * 192 + (e4 >> 1) * 4;
        const char* const f8b = FT + (e8 & 1) * 192 + (e8 >> 1) * 4;
        auto ldTf = [&](const char* bp, int ka, int d) {
            const unsigned* pa_ = (const unsigned*)(bp + ka * 384 - 32 * d); const unsigned* pb_ = (const unsigned*)(bp + (ka + 1) * 384 - 32 * d);
            return (v4u){pa_[0], pa_[1], pb_[0], pb_[1]};
        };
        const f4 z4 = {0, 0, 0, 0};
        auto pkW = [&](f4 a, f4 b) { return (v4u){pkh(a[0], a[1]), pkh(a[2], a[3]), pkh(b[0], b[1]), pkh(b[2], b[3])}; };
        f4 F[8];
#pragma unroll
        for (int t = 2; t < 8; t++) F[t] = z4;
        {
            v4u t01[3], t23[3], t45[4];
#pragma unroll
            for (int d = 0; d < 3; d++) { t01[d] = ldTf(f4b, 0, d + 2); t23[d] = ldTf(f4b, 2, d + 2); }
#pragma unroll
            for (int d = 0; d < 4; d++) t45[d] = ldTf(f8b, 4, d + 4);
#pragma unroll
            for (int tp = 0; tp < 6; tp++) {
                const v4u ua = ldUa(tp), ub = ldUb(tp);
                const v4u w01 = pkW(MFMA16(as_h8(ua), as_h8(bd0), z4), MFMA16(as_h8(ua), as_h8(bd1), z4));
                const v4u w23 = pkW(MFMA16(as_h8(ua), as_h8(bd2), z4), MFMA16(as_h8(ub), as_h8(bd3), z4));
#pragma unroll
                for (int d = 2; d <= 4; d++) if (tp + d < 8) {
                    F[tp + d] = MFMA16(as_h8(w01), as_h8(t01[d - 2]), F[tp + d]);
                    F[tp + d] = MFMA16(as_h8(w23), as_h8(t23[d - 2]), F[tp + d]);
                }
                if (tp < 4) {
                    const v4u w45 = pkW(MFMA16(as_h8(ua), as_h8(bd4), z4), MFMA16(as_h8(ub), as_h8(bd5), z4));
#pragma unroll
                    for (int d = 4; d <= 7; d++) if (tp + d < 8) F[tp + d] = MFMA16(as_h8(w45), as_h8(t45[d - 4]), F[tp + d]);
                    F[tp + 4] = MFMA16(as_h8(an), as_h8(ua), F[tp + 4]);
                }
            }
        }
        {
            const int s0 = 4 * l15 + g4;
            *(f4*)(YB + (s0 + (s0 >> 3)) * 16) = z4; *(f4*)(YB + (72 + s0 + (s0 >> 3)) * 16) = z4;
#pragma unroll
            for (int t = 2; t < 8; t++) *(f4*)(YB + (72 * t + s0 + (s0 >> 3)) * 16) = F[t];
        }
        STAMP(8);
        const int rR = 16 * ((l31 >> 2) & 1) + 4 * (l31 >> 3) + (l31 & 3);
        const int tau = rR + 8 * hh + 1;
        const char* const kb = KD + (tau & 1) * KDO + (tau >> 1) * 4;
        const char* const ub0 = U + (5 * l31 - hh + 85) * 16;
        char* const yw = YB + (9 * l31 + 4 * hh) * 16;
        const int ysl = (T + (T >> 3)) * 16;
        unsigned idx = 0;
#pragma unroll
        for (int be = 0; be < 2; be++) {
            f16v acc;
#pragma unroll
            for (int g = 0; g < 4; g++) { const f4 fv = *(const f4*)(yw + be * 288 * 16 + g * 16); acc[4 * g] = fv[0]; acc[4 * g + 1] = fv[1]; acc[4 * g + 2] = fv[2]; acc[4 * g + 3] = fv[3]; }
#pragma unroll
            for (int j = 0; j < 35; j++) {
                const int t = 3 - 2 * j;
                const int fl = (t >= 0) ? (t >> 2) : -((-t + 3) >> 2);
                const v4u ka = ld4dw(kb + 32 * j);
                const v4u ub = *(const v4u*)(ub0 + (be * 160 + t + fl) * 16);
                acc = MFMA32(as_h8(ka), as_h8(ub), acc);
            }
#pragma unroll
            for (int g = 0; g < 4; g++) *(f4*)(yw + be * 288 * 16 + g * 16) = (f4){acc[4 * g] * osc, acc[4 * g + 1] * osc, acc[4 * g + 2] * osc, acc[4 * g + 3] * osc};
            lds_arrive(lds, C_Y0 + be, lane);
            STAMP(9 + 2 * be);
            if (be == 0) idx = lds_wait_ge(lds, C_IDX, 1) - 1;
            lds_wait_ge(lds, C_Y0 + be, 4);
            f4 yv[4];
#pragma unroll
            for (int c = 0; c < 4; c++) yv[c] = *(const f4*)(lds + L_XY + c * XYB + be * 288 * 16 + ysl);
#pragma unroll
            for (int r = 0; r < 4; r++) {
                const int row = 1024 * be + 4 * T + r;
                const float o0 = yv[0][r] + base[4 * be + r][0], o1 = yv[1][r] + base[4 * be + r][1], o2 = yv[2][r] + base[4 * be + r][2], o3 = yv[3][r] + base[4 * be + r][3];
                if (row != (int)idx) __builtin_amdgcn_raw_buffer_store_b64((v2u){pkbf(o0, o1), pkbf(o2, o3)}, rout, row * 2048 + cg * 8, 0, 0);
                else {
                    *(f4*)(lds + L_CT + C_ROWV * 4) = (f4){o0, o1, o2, o3};
                    asm volatile("s_waitcnt lgkmcnt(0)" ::: "memory");
                    __hip_atomic_store((unsigned*)(lds + L_CT) + C_ROWF, 1u, __ATOMIC_RELAXED, __HIP_MEMORY_SCOPE_WORKGROUP);
                }
            }
            STAMP(10 + 2 * be);
        }
        STAMP(13);
        SFLUSH(A.ws, 1, 8);
    } else {
        const int wv = wave - 4;
        const bool moe = (b >> 3) < 28;
        const int mo = (b & 7) * 28 + (b >> 3), fb = mo >> 2, kq = mo & 3;
        const v4u cnd = __builtin_amdgcn_raw_buffer_load_b128(rws, WS_CAND + lane * 16, 0, 0);
        if (wv == 0 && lane < 32) ((unsigned*)(lds + L_CT))[lane] = 0;
        asm volatile("s_waitcnt lgkmcnt(0)" ::: "memory");
        __builtin_amdgcn_s_barrier();
        STAMP(1);
        if (!moe) {
            if (wv != 0) return;
        }
        const int k0 = kq * 256 + wv * 64 + (lane >> 3) * 8;
        v4u wgv[8], wuv[8];
        {
            auto rg = mkrs(A.wg, moe ? DM * FF * 2 : 0), ru = mkrs(A.wu, moe ? DM * FF * 2 : 0);
#pragma unroll
            for (int i = 0; i < 8; i++) {
                wgv[i] = __builtin_amdgcn_raw_buffer_load_b128(rg, (k0 + i) * 7168 + fb * 128 + (lane & 7) * 16, 0, AUX_NT);
                wuv[i] = __builtin_amdgcn_raw_buffer_load_b128(ru, (k0 + i) * 7168 + fb * 128 + (lane & 7) * 16, 0, AUX_NT);
            }
        }
        const unsigned cn0 = cnd.x, cn1 = cnd.y, cn2 = cnd.z, cn3 = cnd.w;
        const unsigned best = __builtin_amdgcn_readfirstlane(wave_umax(max(max(cn0, cn1), max(cn2, cn3))));
        const unsigned idx = 2047 - (best & 0xffffu);
        const unsigned key = best >> 16;
        const float rsel = bflo((key & 0x8000u) ? (key & 0x7fffu) : (~key & 0xffffu));
        if (wv == 0 && lane == 0) __hip_atomic_store((unsigned*)(lds + L_CT) + C_IDX, idx + 1, __ATOMIC_RELAXED, __HIP_MEMORY_SCOPE_WORKGROUP);
        bool fin = (wv == 0);
        STAMP(2);
        if (moe) {
            auto rx = mkrs(A.x, LSEQ * DM * 2);
            const v4u xs = __builtin_amdgcn_raw_buffer_load_b128(rx, idx * 2048 + k0 * 2, 0, 0);
            const unsigned ms = __builtin_amdgcn_raw_buffer_load_b32(rws, WS_ST + idx * 4, 0, 0);
            v2u wdv[16];
            auto rd = mkrs(A.wd, DM * FF * 2);
#pragma unroll
            for (int i = 0; i < 16; i++) wdv[i] = __builtin_amdgcn_raw_buffer_load_b64(rd, (fb * 64 + wv * 16 + i) * 2048 + kq * 512 + lane * 8, 0, AUX_NT);
            const float m = bflo(ms), rinv = __builtin_amdgcn_rcpf(bfhi(ms));
            float acc[16];
#pragma unroll
            for (int c = 0; c < 16; c++) acc[c] = 0;
#pragma unroll
            for (int i2 = 0; i2 < 4; i2++) {
                const unsigned dp = pkbf(bflo(xs[i2]) - m, bfhi(xs[i2]) - m);
                const unsigned np = pkbf(bflo(dp) * rinv, bfhi(dp) * rinv);
#pragma unroll
                for (int s = 0; s < 2; s++) {
                    const float xk = s ? bfhi(np) : bflo(np);
                    const int i = 2 * i2 + s;
#pragma unroll
                    for (int c = 0; c < 4; c++) {
                        acc[2 * c] = fmaf(xk, bflo(wgv[i][c]), acc[2 * c]); acc[2 * c + 1] = fmaf(xk, bfhi(wgv[i][c]), acc[2 * c + 1]);
                        acc[8 + 2 * c] = fmaf(xk, bflo(wuv[i][c]), acc[8 + 2 * c]); acc[8 + 2 * c + 1] = fmaf(xk, bfhi(wuv[i][c]), acc[8 + 2 * c + 1]);
                    }
                }
            }
            STAMP(3);
            float r8[8], r4[4], r2[2];
#pragma unroll
            for (int c = 0; c < 8; c++) { f2 t = swap32(acc[c], acc[8 + c]); r8[c] = t.x + t.y; }
#pragma unroll
            for (int c = 0; c < 4; c++) { f2 t = swap16(r8[c], r8[4 + c]); r4[c] = t.x + t.y; }
            const bool b3 = lane & 8;
#pragma unroll
            for (int c = 0; c < 2; c++) { const float keep = b3 ? r4[2 + c] : r4[c], give = b3 ? r4[c] : r4[2 + c]; r2[c] = keep + dpp_ror8(give); }
            const int vi = (lane >> 5) * 64 + 8 * (lane & 7) + 4 * ((lane >> 4) & 1) + 2 * ((lane >> 3) & 1);
            *(f2*)(lds + L_GUR + wv * 512 + vi * 4) = (f2){r2[0], r2[1]};
            if (lds_arrive_rtn(lds, C_GU, lane) == 3) {
                const f2 s0 = *(const f2*)(lds + L_GUR + lane * 8), s1 = *(const f2*)(lds + L_GUR + 512 + lane * 8), s2 = *(const f2*)(lds + L_GUR + 1024 + lane * 8), s3 = *(const f2*)(lds + L_GUR + 1536 + lane * 8);
                const f2 s = (s0 + s1) + (s2 + s3);
                __builtin_amdgcn_raw_buffer_store_b64((v2u){__float_as_uint(s.x) | 1u, __float_as_uint(s.y) | 1u}, rws, WS_GU + mo * 512 + lane * 8, 0, AUX_SC1);
            }
            STAMP(4);
            unsigned gq, uq;
            _Pragma("nounroll") for (unsigned s = 0; s < SPIN_CAP; s++) {
                gq = __builtin_amdgcn_raw_buffer_load_b32(rws, WS_GU + (fb * 4 + (lane >> 4)) * 512 + (16 * wv + (lane & 15)) * 4, 0, AUX_SC1);
                uq = __builtin_amdgcn_raw_buffer_load_b32(rws, WS_GU + (fb * 4 + (lane >> 4)) * 512 + 256 + (16 * wv + (lane & 15)) * 4, 0, AUX_SC1);
                if (__all((gq & uq & 1u) != 0)) break;
                __builtin_amdgcn_s_sleep(2);
            }
            STAMP(5);
            const float gt = rbf(sum32(sum16(__uint_as_float(gq)))), ut = rbf(sum32(sum16(__uint_as_float(uq))));
            const float hv = rbf(rbf(gt / (1.0f + expf(-gt))) * ut);
            float p4[4] = {0, 0, 0, 0};
#pragma unroll
            for (int i = 0; i < 16; i++) {
                const float hs = __uint_as_float(__builtin_amdgcn_readlane(__float_as_uint(hv), i));
                p4[0] = fmaf(hs, bflo(wdv[i].x), p4[0]); p4[1] = fmaf(hs, bfhi(wdv[i].x), p4[1]);
                p4[2] = fmaf(hs, bflo(wdv[i].y), p4[2]); p4[3] = fmaf(hs, bfhi(wdv[i].y), p4[3]);
            }
            *(f4*)(lds + L_PR + wv * 1024 + lane * 16) = (f4){p4[0], p4[1], p4[2], p4[3]};
            fin = false;
            if (lds_arrive_rtn(lds, C_P, lane) == 3) {
                const f4 s0 = *(const f4*)(lds + L_PR + lane * 16), s1 = *(const f4*)(lds + L_PR + 1024 + lane * 16), s2 = *(const f4*)(lds + L_PR + 2048 + lane * 16), s3 = *(const f4*)(lds + L_PR + 3072 + lane * 16);
                const f4 s = (s0 + s1) + (s2 + s3);
                __builtin_amdgcn_raw_buffer_store_b128((v4u){__float_as_uint(s[0]) | 1u, __float_as_uint(s[1]) | 1u, __float_as_uint(s[2]) | 1u, __float_as_uint(s[3]) | 1u},
                                                       rws, WS_P + fb * 4096 + kq * 1024 + lane * 16, 0, AUX_SC1);
                fin = true;
            }
        }
        STAMP(6);
        if (fin) {
            v4u pv;
            const int pr = lane < 56 ? lane : 55;
            _Pragma("nounroll") for (unsigned s = 0; s < SPIN_CAP; s++) {
                pv = __builtin_amdgcn_raw_buffer_load_b128(rws, WS_P + pr * 4096 + cg * 16, 0, AUX_SC1);
                if (__all((pv.x & pv.y & pv.z & pv.w & 1u) != 0)) break;
                __builtin_amdgcn_s_sleep(2);
            }
            STAMP(7);
            float y4[4];
#pragma unroll
            for (int c = 0; c < 4; c++) y4[c] = wave_sum(lane < 56 ? __uint_as_float(pv[c]) : 0.0f);
            const float rs = rbf(1.0f / (1.0f + expf(-rsel)));
            lds_wait_ge(lds, C_ROWF, 1);
            const f4 rv = *(const f4*)(lds + L_CT + C_ROWV * 4);
            float o[4];
#pragma unroll
            for (int c = 0; c < 4; c++) o[c] = rv[c] + rbf(rbf(y4[c]) * rs);
            if (lane == 0) __builtin_amdgcn_raw_buffer_store_b64((v2u){pkbf(o[0], o[1]), pkbf(o[2], o[3])}, rout, idx * 2048 + cg * 8, 0, 0);
            STAMP(8);
        }
        SFLUSH(A.ws, 1, 8);
    }
}

extern "C" void kernel_launch(void* const* d_in, const int* in_sizes, int n_in,
                              void* d_out, int out_size, void* d_ws, size_t ws_size,
                              hipStream_t stream) {
    K2Args a;
    a.x = (const u16*)d_in[0]; a.lar = (const u16*)d_in[1]; a.aim = (const u16*)d_in[2]; a.cre = (const u16*)d_in[3]; a.cim = (const u16*)d_in[4];
    a.ldt = (const u16*)d_in[5]; a.dsk = (const u16*)d_in[6]; a.wg = (const u16*)d_in[9]; a.wu = (const u16*)d_in[10]; a.wd = (const u16*)d_in[11];
    a.out = (u16*)d_out; a.ws = (char*)d_ws;
    k1_ln<<<256, 256, 0, stream>>>(a.x, (const u16*)d_in[8], a.ws);
    k2_main<<<256, 512, 0, stream>>>(a);
}
```

```cpp
#include <hip/hip_runtime.h>
#include <stdint.h>

typedef unsigned short u16;
typedef unsigned v4u __attribute__((ext_vector_type(4)));
typedef unsigned v2u __attribute__((ext_vector_type(2)));
typedef float f4 __attribute__((ext_vector_type(4)));
typedef float f2 __attribute__((ext_vector_type(2)));
typedef float f16v __attribute__((ext_vector_type(16)));
typedef _Float16 h8 __attribute__((ext_vector_type(8)));
typedef _Float16 h2 __attribute__((ext_vector_type(2)));
typedef __bf16 b2 __attribute__((ext_vector_type(2)));
#define DI __device__ __forceinline__
#define LAS __attribute__((address_space(3)))
#define AUX_NT 2
#define AUX_SC1 16

constexpr int LSEQ = 2048, DM = 1024, FF = 3584;
constexpr unsigned WS_EPOCH = 0;
constexpr unsigned WS_CAND = 4096;
constexpr unsigned WS_GU = 16384;
constexpr unsigned WS_P = WS_GU + 224 * 1024;
constexpr unsigned WS_XT = 1u << 20;
constexpr unsigned WS_BYTES = WS_XT + (8u << 20);
constexpr unsigned SPIN_CAP = 1u << 16;

DI float bflo(unsigned v) { return __uint_as_float(v << 16); }
DI float bfhi(unsigned v) { return __uint_as_float(v & 0xffff0000u); }
DI unsigned pkbf(float a, float b) { b2 v = {(__bf16)a, (__bf16)b}; return __builtin_bit_cast(unsigned, v); }
DI b2 asb2(unsigned v) { return __builtin_bit_cast(b2, v); }
DI float dot2(unsigned a, unsigned b, float c) { return __builtin_amdgcn_fdot2_f32_bf16(asb2(a), asb2(b), c, false); }
DI float rbf(float a) { return bflo(pkbf(a, a)); }
DI unsigned pkh(float a, float b) { h2 v = {(_Float16)a, (_Float16)b}; return __builtin_bit_cast(unsigned, v); }
template <int C> DI float dppf(float x) { return __int_as_float(__builtin_amdgcn_update_dpp(0, __float_as_int(x), C, 0xf, 0xf, true)); }
template <int C> DI unsigned dppu(unsigned x) { return (unsigned)__builtin_amdgcn_update_dpp(0, (int)x, C, 0xf, 0xf, true); }
DI f2 swap16(float a, float b) { auto r = __builtin_amdgcn_permlane16_swap(__float_as_uint(a), __float_as_uint(b), false, false); return (f2){__uint_as_float(r[0]), __uint_as_float(r[1])}; }
DI f2 swap32(float a, float b) { auto r = __builtin_amdgcn_permlane32_swap(__float_as_uint(a), __float_as_uint(b), false, false); return (f2){__uint_as_float(r[0]), __uint_as_float(r[1])}; }
DI float sum16(float a) { f2 r = swap16(a, a); return r.x + r.y; }
DI float sum32(float a) { f2 r = swap32(a, a); return r.x + r.y; }
DI float wave_sum(float v) { v += dppf<0x121>(v); v += dppf<0x122>(v); v += dppf<0x124>(v); v += dppf<0x128>(v); return sum32(sum16(v)); }
DI float wave_max(float v) {
    v = fmaxf(v, dppf<0x121>(v)); v = fmaxf(v, dppf<0x122>(v)); v = fmaxf(v, dppf<0x124>(v)); v = fmaxf(v, dppf<0x128>(v));
    f2 r = swap16(v, v); v = fmaxf(r.x, r.y); r = swap32(v, v); return fmaxf(r.x, r.y);
}
DI unsigned wave_umax(unsigned v) {
    v = max(v, dppu<0x121>(v)); v = max(v, dppu<0x122>(v)); v = max(v, dppu<0x124>(v)); v = max(v, dppu<0x128>(v));
    auto r = __builtin_amdgcn_permlane16_swap(v, v, false, false); v = max(r[0], r[1]);
    r = __builtin_amdgcn_permlane32_swap(v, v, false, false); return max(r[0], r[1]);
}
DI float fexp(float x) {
    const float t = x * 1.4426950408889634f;
    const float lo = fmaf(x, 1.4426950408889634f, -t) + x * 1.925963033500011e-8f;
    const float e = __builtin_amdgcn_exp2f(t);
    return fmaf(e, lo * 0.6931471805599453f, e);
}
DI __amdgpu_buffer_rsrc_t mkrs(const void* p, unsigned bytes) { return __builtin_amdgcn_make_buffer_rsrc((void*)p, 0, bytes, 0x00020000); }

#define SDECL
#define STAMP(i)
#define SFLUSH(wsp)
#define SWAITVM

constexpr int UB = 6464;
constexpr int L_U = 0;
constexpr int XYB = 53 * 512;
constexpr int L_XY = 4 * UB;
constexpr int O_YD = 0, O_YF = 9216, O_KD = 18432, KDS = 1344, O_FT = O_KD + 4 * KDS;
static_assert(O_FT + 2304 <= XYB, "region");
constexpr int L_GUR = L_XY + 4 * XYB;
constexpr int L_PR = L_GUR + 2048;
constexpr int L_BD = L_PR + 4096;
constexpr int L_CT = L_BD + 3072;
constexpr int L_TOT = L_CT + 256;
enum { C_LN = 0, C_CMAX = 1, C_CN = 2, C_U = 3, C_Y0 = 4, C_Y1 = 5, C_GU = 6, C_P = 7, C_IDX = 8, C_ROWF = 9, C_TAG = 10, C_TAGF = 11, C_X = 12, C_T = 16, C_K1024 = 20, C_OSC = 24, C_ROWV = 28, C_ST = 32 };

struct KArgs {
    const u16* x; const u16* lar; const u16* aim; const u16* cre; const u16* cim; const u16* ldt; const u16* dsk; const u16* wr;
    const u16* wg; const u16* wu; const u16* wd; u16* out; char* ws;
};

DI unsigned* ctl(char* lds, int w) { return (unsigned*)(lds + L_CT) + w; }
DI void lds_arrive(char* lds, int word, int lane) {
    asm volatile("s_waitcnt lgkmcnt(0)" ::: "memory");
    if (lane == 0) __hip_atomic_fetch_add(ctl(lds, word), 1u, __ATOMIC_RELAXED, __HIP_MEMORY_SCOPE_WORKGROUP);
}
DI unsigned lds_arrive_rtn(char* lds, int word, int lane) {
    asm volatile("s_waitcnt lgkmcnt(0)" ::: "memory");
    unsigned r = 0;
    if (lane == 0) r = __hip_atomic_fetch_add(ctl(lds, word), 1u, __ATOMIC_RELAXED, __HIP_MEMORY_SCOPE_WORKGROUP);
    r = __builtin_amdgcn_readfirstlane(r);
    asm volatile("" ::: "memory");
    return r;
}
DI unsigned lds_wait_ge(char* lds, int word, unsigned want) {
    unsigned v;
    _Pragma("nounroll") for (unsigned s = 0; s < (1u << 22); s++) {
        v = __builtin_amdgcn_readfirstlane(__hip_atomic_load(ctl(lds, word), __ATOMIC_RELAXED, __HIP_MEMORY_SCOPE_WORKGROUP));
        if (v >= want) break;
        __builtin_amdgcn_s_sleep(1);
    }
    asm volatile("" ::: "memory");
    return v;
}
DI f2 cmul(f2 a, f2 b) { f2 t = a * (f2){b.x, b.x}; return __builtin_elementwise_fma((f2){-a.y, a.x}, (f2){b.y, b.y}, t); }
DI f2 cexp_p(float ar2, float air, float p) {
    const float m = __builtin_amdgcn_exp2f(ar2 * p);
    const float t = __builtin_amdgcn_fractf(air * p);
    return (f2){m * __builtin_amdgcn_cosf(t), m * __builtin_amdgcn_sinf(t)};
}
DI h8 as_h8(v4u v) { return __builtin_bit_cast(h8, v); }
DI v4u band_frag(int v0, int width) {
    const unsigned long long m = ((1ull << width) - 1) << 32;
    const unsigned m8 = (unsigned)(m >> (v0 + 32));
    v4u r;
#pragma unroll
    for (int k = 0; k < 4; k++) { const unsigned t = m8 >> (2 * k); r[k] = ((t & 1u) | ((t & 2u) << 15)) * 0x3C00u; }
    return r;
}
DI unsigned opq(unsigned v) { asm volatile("" : "+v"(v)); return v; }
template <typename T> DI T ldsld(unsigned a, int off) { return *(const LAS T*)(size_t)(a + off); }
template <typename T> DI void ldsst(unsigned a, int off, T v) { *(LAS T*)(size_t)(a + off) = v; }
#define MFMA32(a, b, c) __builtin_amdgcn_mfma_f32_32x32x16_f16((a), (b), (c), 0, 0, 0)
#define MFMA16(a, b, c) __builtin_amdgcn_mfma_f32_16x16x32_f16((a), (b), (c), 0, 0, 0)

__global__ void __launch_bounds__(1024, 4) k_main(KArgs A) {
    __shared__ __attribute__((aligned(16))) char lds[L_TOT];
    const int b = blockIdx.x, tid = threadIdx.x, lane = tid & 63;
    const int wave = __builtin_amdgcn_readfirstlane(tid >> 6);
    const int cg = (b & 7) * 32 + (b >> 3);
    const unsigned ldsb = (unsigned)(size_t)(LAS char*)lds;
    SDECL;
    auto rws = mkrs(A.ws, WS_BYTES);
    auto rout = mkrs(A.out, LSEQ * DM * 2);
    const int l31 = lane & 31, hh = lane >> 5, l15 = lane & 15, g4 = lane >> 4;
    const float LOG2E = 1.4426950408889634f, INV2PI = 0.15915494309189535f;
    if (wave >= 8) {
        const int hw = wave - 8, T = tid - 512;
        __builtin_amdgcn_s_setprio(3);
        const int myrow = 32 * (b >> 2) + (b & 3) + 4 * hw;
        auto rx = mkrs(A.x, LSEQ * DM * 2);
        auto rw = mkrs(A.wr, DM * 2);
        v4u xv[2], wv[2];
        xv[0] = __builtin_amdgcn_raw_buffer_load_b128(rx, myrow * 2048 + lane * 16, 0, 0);
        xv[1] = __builtin_amdgcn_raw_buffer_load_b128(rx, myrow * 2048 + 1024 + lane * 16, 0, 0);
        wv[0] = __builtin_amdgcn_raw_buffer_load_b128(rw, lane * 16, 0, 0);
        wv[1] = __builtin_amdgcn_raw_buffer_load_b128(rw, 1024 + lane * 16, 0, 0);
        unsigned ep = __builtin_amdgcn_raw_buffer_load_b32(rws, WS_EPOCH, 0, AUX_SC1);
        v2u dsk2 = *(const v2u*)(A.dsk + cg * 4);
        __builtin_amdgcn_sched_barrier(0);
        __builtin_amdgcn_s_barrier();
        asm volatile("" : "+v"(xv[0]), "+v"(xv[1]));
        STAMP(1);
        *(v4u*)(lds + L_U + hw * 2112 + lane * 16) = xv[0];
        *(v4u*)(lds + L_U + hw * 2112 + 1024 + lane * 16) = xv[1];
        f2 f[8];
#pragma unroll
        for (int e = 0; e < 8; e++) f[e] = (f2){bflo(xv[e >> 2][e & 3]), bfhi(xv[e >> 2][e & 3])};
        f2 s2 = ((f[0] + f[1]) + (f[2] + f[3])) + ((f[4] + f[5]) + (f[6] + f[7]));
        const float mean = wave_sum(s2.x + s2.y) * (1.0f / 1024);
        f2 q2 = {0, 0};
#pragma unroll
        for (int e = 0; e < 8; e++) { const f2 d = f[e] - mean; q2 = __builtin_elementwise_fma(d, d, q2); }
        const float var = wave_sum(q2.x + q2.y) * (1.0f / 1024);
        const float mbf = rbf(mean), sd = rbf(__builtin_amdgcn_sqrtf(rbf(rbf(var) + 1e-5f)));
        const unsigned ms = (__float_as_uint(mbf) >> 16) | (__float_as_uint(sd) & 0xffff0000u);
        if (lane == 0) *ctl(lds, C_ST + hw) = ms;
        lds_arrive(lds, C_LN, lane);
        STAMP(2);
        lds_wait_ge(lds, C_LN, 8);
        asm volatile("" : "+v"(ep), "+v"(dsk2), "+v"(wv[0]), "+v"(wv[1]));
        const unsigned tag = ep + 1u, tag16 = tag & 0xffffu;
        {
            const unsigned m = *ctl(lds, C_ST + (lane & 7));
            const unsigned w1 = (m << 16) | tag16, w3 = (m & 0xffff0000u) | tag16;
#pragma unroll
            for (int i = 0; i < 4; i++) {
                const int cgx = (T >> 3) + 64 * i;
                const v2u x4 = *(const v2u*)(lds + L_U + (lane & 7) * 2112 + cgx * 8);
                __builtin_amdgcn_raw_buffer_store_b128((v4u){x4.x, w1, x4.y, w3}, rws, WS_XT + (cgx * 256 + ((b + cgx) & 255)) * 128 + (lane & 7) * 16, 0, AUX_SC1);
            }
        }
        if (hw == 0) { if (lane == 0) *ctl(lds, C_TAG) = tag; lds_arrive(lds, C_TAGF, lane); }
        STAMP(3);
        {
            const float rinv = __builtin_amdgcn_rcpf(sd);
            float acc = 0;
#pragma unroll
            for (int e = 0; e < 8; e++) {
                const f2 d = f[e] - mbf;
                const unsigned dp = pkbf(d.x, d.y);
                const f2 n = (f2){bflo(dp), bfhi(dp)} * rinv;
                acc = dot2(pkbf(n.x, n.y), wv[e >> 2][e & 3], acc);
            }
            const float r = wave_sum(acc);
            const unsigned rb = pkbf(r, r) & 0xffffu;
            const unsigned key = (rb & 0x8000u) ? (~rb & 0xffffu) : (rb | 0x8000u);
            if (lane == 0) __hip_atomic_fetch_max(ctl(lds, C_CMAX), (key << 16) | (unsigned)(2047 - myrow), __ATOMIC_RELAXED, __HIP_MEMORY_SCOPE_WORKGROUP);
            if (lds_arrive_rtn(lds, C_CN, lane) == 7) {
                const unsigned k2 = *ctl(lds, C_CMAX);
                const unsigned mb = *ctl(lds, C_ST + (((2047 - (k2 & 0xffffu)) >> 2) & 7));
                if (lane == 0) __builtin_amdgcn_raw_buffer_store_b128((v4u){k2, tag, mb, tag}, rws, WS_CAND + b * 16, 0, AUX_SC1);
            }
        }
        __builtin_amdgcn_s_setprio(0);
        STAMP(4);
        v4u bd[3];
        if (hw >= 4) {
            const int v = 8 * g4 + l15;
            bd[0] = band_frag(v - 18, 3); bd[1] = band_frag(v - 21, 5); bd[2] = band_frag(v - 26, 3);
            v4u an = band_frag(v - 18, 2);
#pragma unroll
            for (int k = 0; k < 4; k++) an[k] = ((an[k] >> 10) & 0x00010001u) * 0xffffu;
            *(v4u*)(lds + L_BD + lane * 16) = band_frag(v - 20, 7); *(v4u*)(lds + L_BD + 1024 + lane * 16) = band_frag(v - 19, 9); *(v4u*)(lds + L_BD + 2048 + lane * 16) = an;
        }
#define POLL_DELAY 8
        __builtin_amdgcn_s_sleep(POLL_DELAY);
        v4u gv[4];
        {
            const int G = 8 * hw + (lane >> 3);
            _Pragma("nounroll") for (unsigned s = 0; s < SPIN_CAP; s++) {
#pragma unroll
                for (int j = 0; j < 4; j++) gv[j] = __builtin_amdgcn_raw_buffer_load_b128(rws, WS_XT + (cg * 256 + ((4 * G + j + cg) & 255)) * 128 + (lane & 7) * 16, 0, AUX_SC1);
                unsigned bad = 0;
#pragma unroll
                for (int j = 0; j < 4; j++) bad |= (gv[j].y ^ tag16) | (gv[j].w ^ tag16);
                if (__all((bad & 0xffffu) == 0)) break;
                __builtin_amdgcn_s_sleep(4);
            }
        }
        __builtin_amdgcn_s_setprio(3);
        STAMP(5);
        float base[4][4];
        {
            const float d1[4] = {(1.0f + bflo(dsk2.x)) * (1.0f / 128), (1.0f + bfhi(dsk2.x)) * (1.0f / 128), (1.0f + bflo(dsk2.y)) * (1.0f / 128), (1.0f + bfhi(dsk2.y)) * (1.0f / 128)};
            float un[4][4];
#pragma unroll
            for (int r = 0; r < 4; r++) {
                const float m = bfhi(gv[r].y), rinv = __builtin_amdgcn_rcpf(bfhi(gv[r].w)) * 128.0f;
#pragma unroll
                for (int p = 0; p < 2; p++) {
                    const unsigned xw = p ? gv[r].z : gv[r].x;
                    const f2 xx = {bflo(xw), bfhi(xw)};
                    const f2 d = xx - m;
                    const unsigned dp = pkbf(d.x, d.y);
                    const f2 n = (f2){bflo(dp), bfhi(dp)} * rinv;
                    const unsigned np = pkbf(n.x, n.y);
                    const f2 u = {bflo(np), bfhi(np)};
                    const f2 bs = __builtin_elementwise_fma(u, (f2){d1[2 * p], d1[2 * p + 1]}, xx);
                    un[r][2 * p] = u.x; un[r][2 * p + 1] = u.y; base[r][2 * p] = bs.x; base[r][2 * p + 1] = bs.y;
                }
            }
            const int qp = (T >> 1) + 68;
            const int off = (qp + (qp >> 2)) * 16 + ((T & 1) ? 0 : 8);
#pragma unroll
            for (int c = 0; c < 4; c++) *(v2u*)(lds + L_U + c * UB + off) = (v2u){pkh(un[3][c], un[2][c]), pkh(un[1][c], un[0][c])};
            if (T < 340) { const int c = T / 85; *(v4u*)(lds + L_U + c * UB + (T - 85 * c) * 16) = (v4u){0, 0, 0, 0}; }
        }
        lds_arrive(lds, C_U, lane);
        STAMP(6);
        if (hw >= 4) {
            const int c = hw - 4;
            char* const U = lds + L_U + c * UB;
            char* const R = lds + L_XY + c * XYB;
            lds_wait_ge(lds, C_T + c, 1);
            const int e4 = 79 - l15 + 4 * g4, e8 = 127 - l15 + 4 * g4;
            const char* const f4b = R + O_FT + (e4 & 1) * 192 + (e4 >> 1) * 4;
            const char* const f8b = R + O_FT + (e8 & 1) * 192 + (e8 >> 1) * 4;
            auto ldTf = [&](const char* bp, int ka, int d) {
                const unsigned* pa_ = (const unsigned*)(bp + ka * 384 - 32 * d); const unsigned* pb_ = (const unsigned*)(bp + (ka + 1) * 384 - 32 * d);
                return (v4u){pa_[0], pa_[1], pb_[0], pb_[1]};
            };
            v4u t01[3], t23[3];
#pragma unroll
            for (int d = 0; d < 3; d++) { t01[d] = ldTf(f4b, 0, d + 2); t23[d] = ldTf(f4b, 2, d + 2); }
            lds_wait_ge(lds, C_U, 8);
            STAMP(7);
            const int qa = 2 * l15 + 1 - g4 + 68, qb = qa - 1;
            const char* const uap = U + (qa + (qa >> 2)) * 16;
            const char* const ubp = U + (qb + (qb >> 2)) * 16;
            const f4 z4 = {0, 0, 0, 0};
            auto pkW = [&](f4 a, f4 b_) { return (v4u){pkh(a[0], a[1]), pkh(a[2], a[3]), pkh(b_[0], b_[1]), pkh(b_[2], b_[3])}; };
            f4 F[8];
#pragma unroll
            for (int t = 2; t < 8; t++) F[t] = z4;
            const int s0 = 4 * l15 + g4;
            char* const fw = R + O_YF + (s0 + (s0 >> 3)) * 16;
#pragma unroll
            for (int tp = 0; tp < 6; tp++) {
                const v4u ua = *(const v4u*)(uap + tp * 640), ub = *(const v4u*)(ubp + tp * 640);
                const v4u w01 = pkW(MFMA16(as_h8(ua), as_h8(bd[0]), z4), MFMA16(as_h8(ua), as_h8(bd[1]), z4));
                const v4u w23 = pkW(MFMA16(as_h8(ua), as_h8(bd[2]), z4), MFMA16(as_h8(ub), as_h8(bd[1]), z4));
#pragma unroll
                for (int d = 2; d <= 4; d++) if (tp + d < 8) {
                    F[tp + d] = MFMA16(as_h8(w01), as_h8(t01[d - 2]), F[tp + d]);
                    F[tp + d] = MFMA16(as_h8(w23), as_h8(t23[d - 2]), F[tp + d]);
                }
                if (tp == 1) {
                    *(f4*)(fw + 72 * 16 * 2) = F[2]; *(f4*)(fw + 72 * 16 * 3) = F[3];
                    lds_arrive(lds, C_Y0, lane);
                    __builtin_amdgcn_s_setprio(1);
                }
            }
            __builtin_amdgcn_sched_barrier(0);
            {
                v4u t45[4];
#pragma unroll
                for (int d = 0; d < 4; d++) t45[d] = ldTf(f8b, 4, d + 4);
                const v4u b4 = *(const v4u*)(lds + L_BD + lane * 16), b5 = *(const v4u*)(lds + L_BD + 1024 + lane * 16);
                const v4u an = *(const v4u*)(lds + L_BD + 2048 + lane * 16) & *ctl(lds, C_K1024 + c);
#pragma unroll
                for (int tp = 0; tp < 4; tp++) {
                    const v4u ua = *(const v4u*)(uap + tp * 640), ub = *(const v4u*)(ubp + tp * 640);
                    const v4u w45 = pkW(MFMA16(as_h8(ua), as_h8(b4), z4), MFMA16(as_h8(ub), as_h8(b5), z4));
                    F[tp + 4] = MFMA16(as_h8(an), as_h8(ua), F[tp + 4]);
#pragma unroll
                    for (int d = 4; d <= 7; d++) if (tp + d < 8) F[tp + d] = MFMA16(as_h8(w45), as_h8(t45[d - 4]), F[tp + d]);
                }
            }
#pragma unroll
            for (int t = 4; t < 8; t++) *(f4*)(fw + 72 * 16 * t) = F[t];
            lds_arrive(lds, C_Y1, lane);
            STAMP(8);
        }
        const unsigned idx = lds_wait_ge(lds, C_IDX, 1) - 1;
        const f4 osc = *(const f4*)ctl(lds, C_OSC);
        __builtin_amdgcn_s_setprio(3);
        lds_wait_ge(lds, hw >= 4 ? C_Y1 : C_Y0, 8);
        STAMP(9);
        {
            const char* const yp = lds + L_XY + (T + (T >> 3)) * 16;
            f4 yv[4];
#pragma unroll
            for (int c = 0; c < 4; c++) yv[c] = *(const f4*)(yp + c * XYB + O_YD);
            if (hw >= 2) {
#pragma unroll
                for (int c = 0; c < 4; c++) yv[c] += *(const f4*)(yp + c * XYB + O_YF);
            }
#pragma unroll
            for (int r = 0; r < 4; r++) {
                const int row = 4 * T + r;
                const float o0 = fmaf(yv[0][r], osc[0], base[r][0]), o1 = fmaf(yv[1][r], osc[1], base[r][1]), o2 = fmaf(yv[2][r], osc[2], base[r][2]), o3 = fmaf(yv[3][r], osc[3], base[r][3]);
                __builtin_amdgcn_raw_buffer_store_b64((v2u){pkbf(o0, o1), pkbf(o2, o3)}, rout, row == (int)idx ? 0x7ffffff0 : row * 2048 + cg * 8, 0, 0);
                if (row == (int)idx) {
                    *(f4*)ctl(lds, C_ROWV) = (f4){o0, o1, o2, o3};
                    asm volatile("s_waitcnt lgkmcnt(0)" ::: "memory");
                    __hip_atomic_store(ctl(lds, C_ROWF), 1u, __ATOMIC_RELAXED, __HIP_MEMORY_SCOPE_WORKGROUP);
                }
            }
        }
        STAMP(10);
        SWAITVM;
        STAMP(11);
        SFLUSH(A.ws);
    } else if (wave < 4) {
        const int ch = cg * 4 + wave;
        unsigned pl = *(const unsigned*)(A.lar + ch * 128 + 2 * lane), pa = *(const unsigned*)(A.aim + ch * 128 + 2 * lane);
        unsigned pd = *(const unsigned*)(A.ldt + (ch & ~1));
        if (wave == 0) ((unsigned*)(lds + L_CT))[lane] = 0;
        asm volatile("s_waitcnt lgkmcnt(0)" ::: "memory");
        __builtin_amdgcn_sched_barrier(0);
        __builtin_amdgcn_s_barrier();
        asm volatile("" : "+v"(pl), "+v"(pa), "+v"(pd));
        const float ldt = (wave & 1) ? bfhi(pd) : bflo(pd);
        STAMP(1);
        char* const R = lds + L_XY + wave * XYB;
        {
            const float dt = rbf(fexp(ldt));
            float ar2[2], air[2];
            ar2[0] = (-rbf(fexp(bflo(pl))) * dt) * LOG2E; ar2[1] = (-rbf(fexp(bfhi(pl))) * dt) * LOG2E;
            air[0] = (bflo(pa) * dt) * INV2PI; air[1] = (bfhi(pa) * dt) * INV2PI;
            STAMP(2);
            f2 w[2], y[4][2];
#pragma unroll
            for (int m = 0; m < 2; m++) {
                w[m] = cexp_p(ar2[m], air[m], 8.0f);
                y[0][m] = (f2){1.0f, 0.0f}; y[1][m] = cexp_p(ar2[m], air[m], 64.0f); y[2][m] = cexp_p(ar2[m], air[m], 128.0f); y[3][m] = cexp_p(ar2[m], air[m], 192.0f);
            }
            const unsigned yb = ldsb + L_XY + wave * XYB + 21 * 512 + (lane & 1) * 8;
            const int sl = lane >> 1;
#pragma unroll 1
            for (int s = 0; s < 8; s++) {
                const unsigned a0 = yb + s * 512 + ((sl ^ s) << 4), a1 = a0 ^ 128;
#pragma unroll
                for (int c = 0; c < 4; c++) {
                    ldsst<v2u>((c & 1) ? a1 : a0, c * 4096, (v2u){pkh(y[c][0].x, y[c][0].y), pkh(y[c][1].x, y[c][1].y)});
                    y[c][0] = cmul(y[c][0], w[0]); y[c][1] = cmul(y[c][1], w[1]);
                }
            }
        }
        STAMP(3);
        lds_wait_ge(lds, C_X + wave, 1);
        STAMP(4);
        f16v kacc;
#pragma unroll
        for (int e = 0; e < 16; e++) kacc[e] = 0.0f;
        {
            const int lp = hh ^ (l31 & 15);
            v4u xa[16], yb[16];
#pragma unroll
            for (int t = 0; t < 16; t++) {
                const int so = ((lp ^ ((2 * t) & 15)) + ((2 * t) & 16)) << 4;
                xa[t] = *(const v4u*)(R + l31 * 512 + so);
                yb[t] = *(const v4u*)(R + (21 + l31) * 512 + so);
                if (t >= 4) kacc = MFMA32(as_h8(xa[t - 4]), as_h8(yb[t - 4]), kacc);
                __builtin_amdgcn_sched_barrier(0);
            }
            const v4u z = {0, 0, 0, 0};
            *(v4u*)(R + O_FT + lane * 16) = z; *(v4u*)(R + O_FT + 1024 + lane * 16) = z; if (lane < 16) *(v4u*)(R + O_FT + 2048 + lane * 16) = z;
            { const int cp = lane >> 4, o = lane & 15; *(v4u*)(R + O_KD + cp * KDS + (o < 5 ? o * 16 : 1072 + (o - 5) * 16)) = z; }
#pragma unroll
            for (int t = 12; t < 16; t++) kacc = MFMA32(as_h8(xa[t]), as_h8(yb[t]), kacc);
        }
        STAMP(5);
        {
            char* const KD = R + O_KD;
            char* const FT = R + O_FT;
            float kv[12];
#pragma unroll
            for (int e = 0; e < 12; e++) kv[e] = kacc[e] * (1.0f / 64);
            const unsigned p01 = pkh(kv[0], kv[1]), p23 = pkh(kv[2], kv[3]), p12 = pkh(kv[1], kv[2]);
            const int o1 = 64 + 16 * l31 + 8 * hh;
            *(v2u*)(KD + o1) = (v2u){p01, p23};
            *(unsigned*)(KD + 2 * KDS + o1 - 4) = p01; *(unsigned*)(KD + 2 * KDS + o1) = p23;
            *(u16*)(KD + KDS + o1 - 2) = (u16)p01; *(unsigned*)(KD + KDS + o1) = p12; *(u16*)(KD + KDS + o1 + 4) = (u16)(p23 >> 16);
            *(u16*)(KD + 3 * KDS + o1 - 6) = (u16)p01; *(unsigned*)(KD + 3 * KDS + o1 - 4) = p12; *(u16*)(KD + 3 * KDS + o1) = (u16)(p23 >> 16);
            const float vn = __shfl_down(kv[4], 1);
            const unsigned q00 = pkh(kv[4], kv[4]), q22 = pkh(kv[6], kv[6]);
            if (hh == 0) {
                const v4u ev = {q00, pkh(kv[5], kv[6]), q22, pkh(kv[7], vn)};
                const v4u od = {pkh(kv[4], kv[5]), q22, pkh(kv[6], kv[7]), pkh(vn, vn)};
                const int o2 = 576 + 16 * l31;
                *(v4u*)(KD + o2) = ev; *(v4u*)(KD + KDS + o2) = od;
                unsigned* const e2 = (unsigned*)(KD + 2 * KDS + o2 - 4); unsigned* const o3 = (unsigned*)(KD + 3 * KDS + o2 - 4);
#pragma unroll
                for (int k = 0; k < 4; k++) { e2[k] = ev[k]; o3[k] = od[k]; }
            }
            if (lane == 0) { *(u16*)(KD + KDS + 574) = (u16)q00; *(u16*)(KD + 3 * KDS + 570) = (u16)q00; *ctl(lds, C_K1024 + wave) = pkh(kv[8], kv[8]); }
            if (lane == 32) {
                const unsigned q0z = q00 & 0xffffu;
                *(v2u*)(KD + 1088) = (v2u){q00, q0z};
                *(unsigned*)(KD + 2 * KDS + 1084) = q00; *(unsigned*)(KD + 2 * KDS + 1088) = q0z;
                *(unsigned*)(KD + KDS + 1088) = q00; *(unsigned*)(KD + 3 * KDS + 1084) = q00;
                *(u16*)(FT + 5 * 384) = (u16)pkh(kv[8], kv[8]);
            }
            const int bo = l31 & 1;
            int a0, a1, st2;
            if (hh) { a0 = (bo ? 1 : 3) * 384 + 2 * (79 - (31 + ((l31 + 1) >> 1))); a1 = (bo ? 2 : 0) * 384 + 2 * (79 - (32 + (l31 >> 1))); st2 = 32; }
            else { a0 = (bo ? 4 : 5) * 384 + 2 * (127 - (63 + ((l31 + 1) >> 1))); a1 = a0 - 32; st2 = 64; }
            int ad[4] = {a0, a1, a0 - st2, a1 - st2};
            if (lane == 0) ad[0] = 3 * 384 + 2 * 16;
            if (lane == 32) ad[0] = 3 * 384 + 2 * 70;
#pragma unroll
            for (int e = 0; e < 4; e++) {
                const u16 hv = (u16)pkh(hh ? kv[4 + e] : kv[8 + e], 0.0f);
                *(u16*)(FT + ad[e]) = hv; *(u16*)(FT + ad[e] + 190) = hv;
            }
        }
        lds_arrive(lds, C_T + wave, lane);
        STAMP(6);
        const int rR = 16 * ((l31 >> 2) & 1) + 4 * (l31 >> 3) + (l31 & 3);
        const int tau = rR + 8 * hh + 1;
        const unsigned ka0 = opq(ldsb + L_XY + wave * XYB + O_KD + (tau & 3) * KDS + (tau >> 2) * 8);
        const unsigned ka1 = opq(ka0 + 8);
        unsigned ub = ldsb + L_U + wave * UB + (5 * l31 - hh + 3) * 16;
        unsigned yw = ldsb + L_XY + wave * XYB + O_YD + (9 * l31 + 4 * hh) * 16;
        constexpr int PD = 4;
        v2u kf0[35], kf1[35];
#pragma unroll
        for (int j = 0; j < PD; j++) { kf0[j] = ldsld<v2u>(ka0, 32 * j); kf1[j] = ldsld<v2u>(ka1, 32 * j); }
        lds_wait_ge(lds, C_U, 8);
        __builtin_amdgcn_s_setprio(2);
        STAMP(7);
#pragma unroll 1
        for (int be = 0; be < 2; be++) {
            auto uoff = [](int j) { const int t = 3 - 2 * j; const int fl = (t >= 0) ? (t >> 2) : -((-t + 3) >> 2); return (82 + t + fl) * 16; };
            v4u uf[35];
#pragma unroll
            for (int j = 0; j < PD; j++) uf[j] = ldsld<v4u>(ub, uoff(j));
            f16v acc;
#pragma unroll
            for (int e = 0; e < 16; e++) acc[e] = 0.0f;
            __builtin_amdgcn_sched_barrier(0);
#pragma unroll
            for (int j = 0; j < 35; j++) {
                acc = MFMA32(as_h8((v4u){kf0[j].x, kf0[j].y, kf1[j].x, kf1[j].y}), as_h8(uf[j]), acc);
                const int jn = (j + PD) % 35;
                kf0[jn] = ldsld<v2u>(ka0, 32 * jn); kf1[jn] = ldsld<v2u>(ka1, 32 * jn);
                if (j + PD < 35) uf[j + PD] = ldsld<v4u>(ub, uoff(j + PD));
                __builtin_amdgcn_sched_barrier(0);
            }
#pragma unroll
            for (int g = 0; g < 4; g++) ldsst<f4>(yw, g * 16, (f4){acc[4 * g], acc[4 * g + 1], acc[4 * g + 2], acc[4 * g + 3]});
            lds_arrive(lds, C_Y0 + be, lane);
            __builtin_amdgcn_s_setprio(1);
            ub += 160 * 16; yw += 288 * 16;
            if (be == 0) STAMP(8); else STAMP(9);
        }
        SFLUSH(A.ws);
    } else {
        const int wv = wave - 4, ch = cg * 4 + wv;
        const bool moe = (b >> 3) < 28;
        const int mo = (b & 7) * 28 + (b >> 3), fb = mo >> 2, kq = mo & 3;
        unsigned pl = *(const unsigned*)(A.lar + ch * 128 + 2 * lane), pa = *(const unsigned*)(A.aim + ch * 128 + 2 * lane);
        unsigned pcr = *(const unsigned*)(A.cre + ch * 128 + 2 * lane), pci = *(const unsigned*)(A.cim + ch * 128 + 2 * lane);
        unsigned pd = *(const unsigned*)(A.ldt + (ch & ~1));
        __builtin_amdgcn_sched_barrier(0);
        __builtin_amdgcn_s_barrier();
        const int k0 = kq * 256 + wv * 64 + (lane >> 3) * 8;
        v4u wgv[8], wuv[8];
        {
            auto rg = mkrs(A.wg, moe ? DM * FF * 2 : 0), ru = mkrs(A.wu, moe ? DM * FF * 2 : 0);
#pragma unroll
            for (int i = 0; i < 8; i++) {
                wgv[i] = __builtin_amdgcn_raw_buffer_load_b128(rg, (k0 + i) * 7168 + fb * 128 + (lane & 7) * 16, 0, AUX_NT);
                wuv[i] = __builtin_amdgcn_raw_buffer_load_b128(ru, (k0 + i) * 7168 + fb * 128 + (lane & 7) * 16, 0, AUX_NT);
            }
        }
        __builtin_amdgcn_sched_barrier(0);
        asm volatile("" : "+v"(pl), "+v"(pa), "+v"(pcr), "+v"(pci), "+v"(pd));
        const float ldt = (wave & 1) ? bfhi(pd) : bflo(pd);
        STAMP(1);
        {
            const float dt = rbf(fexp(ldt));
            float ar2[2], air[2];
            ar2[0] = (-rbf(fexp(bflo(pl))) * dt) * LOG2E; ar2[1] = (-rbf(fexp(bfhi(pl))) * dt) * LOG2E;
            air[0] = (bflo(pa) * dt) * INV2PI; air[1] = (bfhi(pa) * dt) * INV2PI;
            float mx = fmaxf(fabsf(bflo(pcr)) + fabsf(bflo(pci)), fabsf(bfhi(pcr)) + fabsf(bfhi(pci))) * (2.0f * dt);
            mx = wave_max(mx);
            int E = (__float_as_uint(mx) >> 23) & 0xff; E = min(max(E, 20), 240);
            const float cs = __uint_as_float((unsigned)(268 - E) << 23);
            if (lane == 0) *ctl(lds, C_OSC + wv) = (unsigned)(E - 14) << 23;
            f2 C[2] = {(f2){bflo(pcr) * dt * cs, -(bflo(pci) * dt * cs)}, (f2){bfhi(pcr) * dt * cs, -(bfhi(pci) * dt * cs)}};
            air[0] = -air[0]; air[1] = -air[1];
            const unsigned xb = ldsb + L_XY + wv * XYB + (lane & 1) * 8;
            const int sl = lane >> 1;
            auto putX = [&](int row, const f2* a) { ldsst<v2u>(xb + ((sl ^ (row & 15)) << 4), row * 512, (v2u){pkh(a[0].x, a[0].y), pkh(a[1].x, a[1].y)}); };
            auto mul2 = [&](f2* o, const f2* a, const f2* m) { o[0] = cmul(a[0], m[0]); o[1] = cmul(a[1], m[1]); };
            f2 z[2], zb[2], z2[2], t[2], p256[2], p258[2], p260[2], p262[2], p512[2], p516[2], p772[2];
#pragma unroll
            for (int m = 0; m < 2; m++) { z[m] = cexp_p(ar2[m], air[m], 1.0f); zb[m] = cexp_p(ar2[m], air[m], 256.0f); z2[m] = cmul(z[m], z[m]); t[m] = C[m]; }
#pragma unroll
            for (int e = 0; e < 8; e++) { putX(e, t); if (e < 7) mul2(t, t, z); }
            mul2(p256, C, zb); mul2(p258, p256, z2); mul2(p260, p258, z2); mul2(p262, p260, z2);
            putX(8, p256); putX(9, p258); putX(10, p260); putX(11, p262);
            mul2(p512, p256, zb); mul2(p516, p260, zb); mul2(t, p512, zb); mul2(p772, p516, zb);
            putX(12, p512); putX(13, p516); putX(14, t); putX(15, p772);
#pragma unroll
            for (int e = 0; e < 5; e++) { mul2(t, t, zb); putX(16 + e, t); }
        }
        lds_arrive(lds, C_X + wv, lane);
        STAMP(2);
        if (!moe && wv != 0) { SFLUSH(A.ws); return; }
        lds_wait_ge(lds, C_TAGF, 1);
        const unsigned tag = *ctl(lds, C_TAG);
        v4u cn[4];
        _Pragma("nounroll") for (unsigned s = 0; s < SPIN_CAP; s++) {
#pragma unroll
            for (int j = 0; j < 4; j++) cn[j] = __builtin_amdgcn_raw_buffer_load_b128(rws, WS_CAND + (lane + 64 * j) * 16, 0, AUX_SC1);
            unsigned bad = 0;
#pragma unroll
            for (int j = 0; j < 4; j++) bad |= (cn[j].y ^ tag) | (cn[j].w ^ tag);
            if (__all(bad == 0)) break;
            __builtin_amdgcn_s_sleep(4);
        }
        __builtin_amdgcn_s_setprio(3);
        STAMP(3);
        const unsigned best = __builtin_amdgcn_readfirstlane(wave_umax(max(max(cn[0].x, cn[1].x), max(cn[2].x, cn[3].x))));
        const unsigned idx = (2047 - (best & 0xffffu)) & 2047u;
        const unsigned key = best >> 16;
        const float rsel = bflo((key & 0x8000u) ? (key & 0x7fffu) : (~key & 0xffffu));
        if (wv == 0 && lane == 0) {
            __hip_atomic_store(ctl(lds, C_IDX), idx + 1, __ATOMIC_RELAXED, __HIP_MEMORY_SCOPE_WORKGROUP);
            if (b == 0) __builtin_amdgcn_raw_buffer_store_b32(tag, rws, WS_EPOCH, 0, AUX_SC1);
        }
        bool fin = true;
        if (moe) {
            auto rx = mkrs(A.x, LSEQ * DM * 2);
            const v4u xs = __builtin_amdgcn_raw_buffer_load_b128(rx, idx * 2048 + k0 * 2, 0, 0);
            v2u wdv[16];
            auto rd = mkrs(A.wd, DM * FF * 2);
#pragma unroll
            for (int i = 0; i < 16; i++) wdv[i] = __builtin_amdgcn_raw_buffer_load_b64(rd, (fb * 64 + wv * 16 + i) * 2048 + kq * 512 + lane * 8, 0, AUX_NT);
            __builtin_amdgcn_sched_barrier(0);
            unsigned msl = 0;
#pragma unroll
            for (int j = 0; j < 4; j++) msl = cn[j].x == best ? cn[j].z : msl;
            const unsigned ms = __builtin_amdgcn_readfirstlane(wave_umax(msl));
            const float m = bflo(ms), rinv = __builtin_amdgcn_rcpf(bfhi(ms));
            float acc[16];
#pragma unroll
            for (int c = 0; c < 16; c++) acc[c] = 0;
#pragma unroll
            for (int i2 = 0; i2 < 4; i2++) {
                const unsigned dp = pkbf(bflo(xs[i2]) - m, bfhi(xs[i2]) - m);
                const unsigned np = pkbf(bflo(dp) * rinv, bfhi(dp) * rinv);
                const unsigned nlo = np & 0xffffu, nlo1 = np << 16, nhi = np >> 16, nhi1 = np & 0xffff0000u;
#pragma unroll
                for (int s = 0; s < 2; s++) {
                    const int i = 2 * i2 + s;
                    const unsigned xl = s ? nhi : nlo, xh = s ? nhi1 : nlo1;
#pragma unroll
                    for (int c = 0; c < 4; c++) {
                        acc[2 * c] = dot2(wgv[i][c], xl, acc[2 * c]);
                        acc[2 * c + 1] = dot2(wgv[i][c], xh, acc[2 * c + 1]);
                        acc[8 + 2 * c] = dot2(wuv[i][c], xl, acc[8 + 2 * c]);
                        acc[8 + 2 * c + 1] = dot2(wuv[i][c], xh, acc[8 + 2 * c + 1]);
                    }
                }
            }
            STAMP(4);
            float r8[8], r4[4], r2[2];
#pragma unroll
            for (int c = 0; c < 8; c++) { f2 t = swap32(acc[c], acc[8 + c]); r8[c] = t.x + t.y; }
#pragma unroll
            for (int c = 0; c < 4; c++) { f2 t = swap16(r8[c], r8[4 + c]); r4[c] = t.x + t.y; }
            const bool b3 = lane & 8;
#pragma unroll
            for (int c = 0; c < 2; c++) { const float keep = b3 ? r4[2 + c] : r4[c], give = b3 ? r4[c] : r4[2 + c]; r2[c] = keep + dppf<0x128>(give); }
            const int vi = (lane >> 5) * 64 + 8 * (lane & 7) + 4 * ((lane >> 4) & 1) + 2 * ((lane >> 3) & 1);
            *(f2*)(lds + L_GUR + wv * 512 + vi * 4) = (f2){r2[0], r2[1]};
            if (lds_arrive_rtn(lds, C_GU, lane) == 3) {
                const f2 s0 = *(const f2*)(lds + L_GUR + lane * 8), s1 = *(const f2*)(lds + L_GUR + 512 + lane * 8), s2 = *(const f2*)(lds + L_GUR + 1024 + lane * 8), s3 = *(const f2*)(lds + L_GUR + 1536 + lane * 8);
                const f2 s = (s0 + s1) + (s2 + s3);
                __builtin_amdgcn_raw_buffer_store_b128((v4u){__float_as_uint(s.x), tag, __float_as_uint(s.y), tag}, rws, WS_GU + mo * 1024 + lane * 16, 0, AUX_SC1);
            }
            STAMP(5);
            v2u gq, uq;
            _Pragma("nounroll") for (unsigned s = 0; s < SPIN_CAP; s++) {
                gq = __builtin_amdgcn_raw_buffer_load_b64(rws, WS_GU + (fb * 4 + g4) * 1024 + (16 * wv + l15) * 8, 0, AUX_SC1);
                uq = __builtin_amdgcn_raw_buffer_load_b64(rws, WS_GU + (fb * 4 + g4) * 1024 + 512 + (16 * wv + l15) * 8, 0, AUX_SC1);
                if (__all(((gq.y ^ tag) | (uq.y ^ tag)) == 0)) break;
                __builtin_amdgcn_s_sleep(2);
            }
            STAMP(6);
            const float gt = rbf(sum32(sum16(__uint_as_float(gq.x)))), ut = rbf(sum32(sum16(__uint_as_float(uq.x))));
            const float hv = rbf(rbf(gt * __builtin_amdgcn_rcpf(1.0f + fexp(-gt))) * ut);
            const unsigned hb = __float_as_uint(hv);
            float p4[4] = {0, 0, 0, 0};
#pragma unroll
            for (int i = 0; i < 16; i++) {
                const unsigned hs = __builtin_amdgcn_readlane(hb, i);
                const unsigned hh1 = hs, hl = hs >> 16;
                p4[0] = dot2(wdv[i].x, hl, p4[0]);
                p4[1] = dot2(wdv[i].x, hh1, p4[1]);
                p4[2] = dot2(wdv[i].y, hl, p4[2]);
                p4[3] = dot2(wdv[i].y, hh1, p4[3]);
            }
            *(f4*)(lds + L_PR + wv * 1024 + lane * 16) = (f4){p4[0], p4[1], p4[2], p4[3]};
            fin = false;
            if (lds_arrive_rtn(lds, C_P, lane) == 3) {
                const f4 s0 = *(const f4*)(lds + L_PR + lane * 16), s1 = *(const f4*)(lds + L_PR + 1024 + lane * 16), s2 = *(const f4*)(lds + L_PR + 2048 + lane * 16), s3 = *(const f4*)(lds + L_PR + 3072 + lane * 16);
                const f4 s = (s0 + s1) + (s2 + s3);
                __builtin_amdgcn_raw_buffer_store_b128((v4u){__float_as_uint(s[0]), tag, __float_as_uint(s[1]), tag}, rws, WS_P + fb * 8192 + kq * 2048 + lane * 32, 0, AUX_SC1);
                __builtin_amdgcn_raw_buffer_store_b128((v4u){__float_as_uint(s[2]), tag, __float_as_uint(s[3]), tag}, rws, WS_P + fb * 8192 + kq * 2048 + lane * 32 + 16, 0, AUX_SC1);
                fin = true;
            }
            STAMP(7);
        }
        if (fin) {
            v4u p0, p1;
            const int pr = lane < 56 ? lane : 55;
            _Pragma("nounroll") for (unsigned s = 0; s < SPIN_CAP; s++) {
                p0 = __builtin_amdgcn_raw_buffer_load_b128(rws, WS_P + pr * 8192 + cg * 32, 0, AUX_SC1);
                p1 = __builtin_amdgcn_raw_buffer_load_b128(rws, WS_P + pr * 8192 + cg * 32 + 16, 0, AUX_SC1);
                if (__all(((p0.y ^ tag) | (p0.w ^ tag) | (p1.y ^ tag) | (p1.w ^ tag)) == 0)) break;
                __builtin_amdgcn_s_sleep(2);
            }
            STAMP(8);
            const bool on = lane < 56;
            const float y0 = wave_sum(on ? __uint_as_float(p0.x) : 0.0f), y1 = wave_sum(on ? __uint_as_float(p0.z) : 0.0f);
            const float y2 = wave_sum(on ? __uint_as_float(p1.x) : 0.0f), y3 = wave_sum(on ? __uint_as_float(p1.z) : 0.0f);
            const float rs = rbf(__builtin_amdgcn_rcpf(1.0f + fexp(-rsel)));
            lds_wait_ge(lds, C_ROWF, 1);
            const f4 rv = *(const f4*)ctl(lds, C_ROWV);
            const float o0 = rv[0] + rbf(rbf(y0) * rs), o1 = rv[1] + rbf(rbf(y1) * rs), o2 = rv[2] + rbf(rbf(y2) * rs), o3 = rv[3] + rbf(rbf(y3) * rs);
            if (lane == 0) __builtin_amdgcn_raw_buffer_store_b64((v2u){pkbf(o0, o1), pkbf(o2, o3)}, rout, idx * 2048 + cg * 8, 0, 0);
            STAMP(9);
        }
        SFLUSH(A.ws);
    }
}

extern "C" void kernel_launch(void* const* d_in, const int* in_sizes, int n_in,
                              void* d_out, int out_size, void* d_ws, size_t ws_size,
                              hipStream_t stream) {
    KArgs a;
    a.x = (const u16*)d_in[0]; a.lar = (const u16*)d_in[1]; a.aim = (const u16*)d_in[2]; a.cre = (const u16*)d_in[3]; a.cim = (const u16*)d_in[4];
    a.ldt = (const u16*)d_in[5]; a.dsk = (const u16*)d_in[6]; a.wr = (const u16*)d_in[8];
    a.wg = (const u16*)d_in[9]; a.wu = (const u16*)d_in[10]; a.wd = (const u16*)d_in[11];
    a.out = (u16*)d_out; a.ws = (char*)d_ws;
    k_main<<<256, 1024, 0, stream>>>(a);
}
```

```cpp
#include <hip/hip_runtime.h>
#include <stdint.h>

typedef unsigned short u16;
typedef unsigned v4u __attribute__((ext_vector_type(4)));
typedef unsigned v2u __attribute__((ext_vector_type(2)));
typedef float f4 __attribute__((ext_vector_type(4)));
typedef float f2 __attribute__((ext_vector_type(2)));
typedef float f16v __attribute__((ext_vector_type(16)));
typedef _Float16 h8 __attribute__((ext_vector_type(8)));
typedef _Float16 h2 __attribute__((ext_vector_type(2)));
typedef __bf16 b2 __attribute__((ext_vector_type(2)));
#define DI __device__ __forceinline__
#define LAS __attribute__((address_space(3)))
#define AUX_NT 2
#define AUX_SC1 16
#define AUX_W 2

constexpr int LSEQ = 2048, DM = 1024, FF = 3584;
constexpr unsigned WS_EPOCH = 0;
constexpr unsigned WS_CAND = 4096;
constexpr unsigned WS_GU = 16384;
constexpr unsigned WS_P = WS_GU + 224 * 1024;
constexpr unsigned WS_XT = 1u << 20;
constexpr unsigned WS_BYTES = WS_XT + (8u << 20);
#define WD1 8
#define H3_SLEEP 8
#define CODE_PF 19456
constexpr unsigned SPIN_CAP = 1u << 16;

DI float bflo(unsigned v) { return __uint_as_float(v << 16); }
DI float bfhi(unsigned v) { return __uint_as_float(v & 0xffff0000u); }
DI unsigned pkbf(float a, float b) { b2 v = {(__bf16)a, (__bf16)b}; return __builtin_bit_cast(unsigned, v); }
DI b2 asb2(unsigned v) { return __builtin_bit_cast(b2, v); }
DI float dot2(unsigned a, unsigned b, float c) { return __builtin_amdgcn_fdot2_f32_bf16(asb2(a), asb2(b), c, false); }
DI float rbf(float a) { return bflo(pkbf(a, a)); }
DI unsigned pkh(float a, float b) { h2 v = {(_Float16)a, (_Float16)b}; return __builtin_bit_cast(unsigned, v); }
template <int C> DI float dppf(float x) { return __int_as_float(__builtin_amdgcn_update_dpp(0, __float_as_int(x), C, 0xf, 0xf, true)); }
template <int C> DI unsigned dppu(unsigned x) { return (unsigned)__builtin_amdgcn_update_dpp(0, (int)x, C, 0xf, 0xf, true); }
DI f2 swap16(float a, float b) { auto r = __builtin_amdgcn_permlane16_swap(__float_as_uint(a), __float_as_uint(b), false, false); return (f2){__uint_as_float(r[0]), __uint_as_float(r[1])}; }
DI f2 swap32(float a, float b) { auto r = __builtin_amdgcn_permlane32_swap(__float_as_uint(a), __float_as_uint(b), false, false); return (f2){__uint_as_float(r[0]), __uint_as_float(r[1])}; }
DI float sum16(float a) { f2 r = swap16(a, a); return r.x + r.y; }
DI float sum32(float a) { f2 r = swap32(a, a); return r.x + r.y; }
DI float wave_sum(float v) { v += dppf<0x121>(v); v += dppf<0x122>(v); v += dppf<0x124>(v); v += dppf<0x128>(v); return sum32(sum16(v)); }
DI float wave_max(float v) {
    v = fmaxf(v, dppf<0x121>(v)); v = fmaxf(v, dppf<0x122>(v)); v = fmaxf(v, dppf<0x124>(v)); v = fmaxf(v, dppf<0x128>(v));
    f2 r = swap16(v, v); v = fmaxf(r.x, r.y); r = swap32(v, v); return fmaxf(r.x, r.y);
}
DI unsigned wave_umax(unsigned v) {
    v = max(v, dppu<0x121>(v)); v = max(v, dppu<0x122>(v)); v = max(v, dppu<0x124>(v)); v = max(v, dppu<0x128>(v));
    auto r = __builtin_amdgcn_permlane16_swap(v, v, false, false); v = max(r[0], r[1]);
    r = __builtin_amdgcn_permlane32_swap(v, v, false, false); return max(r[0], r[1]);
}
DI float fexp(float x) {
    const float t = x * 1.4426950408889634f;
    const float lo = fmaf(x, 1.4426950408889634f, -t) + x * 1.925963033500011e-8f;
    const float e = __builtin_amdgcn_exp2f(t);
    return fmaf(e, lo * 0.6931471805599453f, e);
}
DI __amdgpu_buffer_rsrc_t mkrs(const void* p, unsigned bytes) { return __builtin_amdgcn_make_buffer_rsrc((void*)p, 0, bytes, 0x00020000); }

#define SDECL
#define STAMP(i)
#define SFLUSH(wsp)
#define SWAITVM

constexpr int UB = 6144;
constexpr int L_U = 0;
constexpr int LNS = 2080;
DI constexpr int uplane(int p) { return p == 0 ? 0 : p == 1 ? 96 : p == 2 ? 200 : 296; }
DI int uslot(int q) { return ((q & 1) ? 96 : 0) + ((q & 2) ? 200 : 0) + (q >> 2); }
constexpr int XYB = 53 * 512;
constexpr int L_XY = 4 * UB;
constexpr int O_YD = 0, O_YF = 9216, O_KD = 18432, KD1 = 1352, KD2 = 2696, KD3 = 4040, O_FT = O_KD + 5376;
static_assert(O_FT + 2304 <= XYB, "region");
constexpr int L_GUR = L_XY + 4 * XYB;
constexpr int L_PR = L_GUR + 2048;
constexpr int L_BD = L_PR + 4096;
constexpr int L_CT = L_BD + 7168;
constexpr int L_TOT = L_CT + 256;
enum { C_LN = 0, C_CMAX = 1, C_CN = 2, C_U = 3, C_Y0 = 4, C_Y1 = 5, C_GU = 6, C_P = 7, C_IDX = 8, C_ROWF = 9, C_TAG = 10, C_SI = 11, C_X = 12, C_T = 16, C_K1024 = 20, C_OSC = 24, C_ROWV = 28, C_ST = 32, C_MS = 40, C_BD = 41 };

struct KArgs {
    const u16* x; const u16* lar; const u16* aim; const u16* cre; const u16* cim; const u16* ldt; const u16* dsk; const u16* wr;
    const u16* wg; const u16* wu; const u16* wd; u16* out; char* ws;
};

DI unsigned* ctl(char* lds, int w) { return (unsigned*)(lds + L_CT) + w; }
DI void lds_arrive(char* lds, int word, int lane) {
    asm volatile("s_mov_b64 exec, 1\n\tds_add_u32 %0, %1\n\ts_mov_b64 exec, -1" :: "v"((unsigned)(size_t)(LAS unsigned*)ctl(lds, word)), "v"(1u) : "memory");
}
DI unsigned lds_arrive_rtn(char* lds, int word, int lane) {
    unsigned r;
    asm volatile("s_mov_b64 exec, 1\n\tds_add_rtn_u32 %0, %1, %2\n\ts_mov_b64 exec, -1\n\ts_waitcnt lgkmcnt(0)" : "=&v"(r) : "v"((unsigned)(size_t)(LAS unsigned*)ctl(lds, word)), "v"(1u) : "memory");
    return __builtin_amdgcn_readfirstlane(r);
}
DI unsigned lds_wait_ge(char* lds, int word, unsigned want) {
    unsigned v;
    _Pragma("nounroll") for (unsigned s = 0; s < (1u << 22); s++) {
        v = __builtin_amdgcn_readfirstlane(__hip_atomic_load(ctl(lds, word), __ATOMIC_RELAXED, __HIP_MEMORY_SCOPE_WORKGROUP));
        if (v >= want) break;
        __builtin_amdgcn_s_sleep(1);
    }
    asm volatile("" ::: "memory");
    return v;
}
DI f2 cmul(f2 a, f2 b) { f2 t = a * (f2){b.x, b.x}; return __builtin_elementwise_fma((f2){-a.y, a.x}, (f2){b.y, b.y}, t); }
DI f2 cexp_p(float ar2, float air, float p) {
    const float m = __builtin_amdgcn_exp2f(ar2 * p);
    const float t = __builtin_amdgcn_fractf(air * p);
    return (f2){m * __builtin_amdgcn_cosf(t), m * __builtin_amdgcn_sinf(t)};
}
DI h8 as_h8(v4u v) { return __builtin_bit_cast(h8, v); }
DI v4u band_frag(int v0, int width, unsigned one) {
    const unsigned long long m = ((1ull << width) - 1) << 32;
    const unsigned m8 = (unsigned)(m >> (v0 + 32));
    v4u r;
#pragma unroll
    for (int k = 0; k < 4; k++) { const unsigned t = m8 >> (2 * k); r[k] = ((t & 1u) | ((t & 2u) << 15)) * one; }
    return r;
}
DI unsigned opq(unsigned v) { asm volatile("" : "+v"(v)); return v; }
template <typename T> DI T ldsld(unsigned a, int off) { return *(const LAS T*)(size_t)(a + off); }
template <typename T> DI void ldsst(unsigned a, int off, T v) { *(LAS T*)(size_t)(a + off) = v; }
#define MFMA32(a, b, c) __builtin_amdgcn_mfma_f32_32x32x16_f16((a), (b), (c), 0, 0, 0)
#define MFMA16(a, b, c) __builtin_amdgcn_mfma_f32_16x16x32_f16((a), (b), (c), 0, 0, 0)

DI void ygen(const float* ar2, const float* air, float p0, unsigned rows, int lane) {
    f2 w[2], y[2][2];
#pragma unroll
    for (int m = 0; m < 2; m++) { w[m] = cexp_p(ar2[m], air[m], 8.0f); y[0][m] = cexp_p(ar2[m], air[m], p0); y[1][m] = cexp_p(ar2[m], air[m], p0 + 64.0f); }
    const unsigned yb = rows + (lane & 1) * 8;
    const int sl = lane >> 1;
#pragma unroll 2
    for (int s = 0; s < 8; s++) {
        const unsigned a0 = yb + s * 512 + ((sl ^ s) << 4), a1 = a0 ^ 128;
        ldsst<v2u>(a0, 0, (v2u){pkh(y[0][0].x, y[0][0].y), pkh(y[0][1].x, y[0][1].y)});
        ldsst<v2u>(a1, 4096, (v2u){pkh(y[1][0].x, y[1][0].y), pkh(y[1][1].x, y[1][1].y)});
#pragma unroll
        for (int c = 0; c < 2; c++) { y[c][0] = cmul(y[c][0], w[0]); y[c][1] = cmul(y[c][1], w[1]); }
    }
}

__global__ void __launch_bounds__(1024, 4) k_main(KArgs A) {
    __shared__ __attribute__((aligned(16))) char lds[L_TOT];
    const int b = blockIdx.x, tid = threadIdx.x, lane = tid & 63;
    const int wave = __builtin_amdgcn_readfirstlane(tid >> 6);
    const int cg = (b & 7) * 32 + (b >> 3);
    const unsigned ldsb = (unsigned)(size_t)(LAS char*)lds;
    SDECL;
    auto rws = mkrs(A.ws, WS_BYTES);
    auto rout = mkrs(A.out, LSEQ * DM * 2);
    const int l31 = lane & 31, hh = lane >> 5, l15 = lane & 15, g4 = lane >> 4;
    const float LOG2E = 1.4426950408889634f, INV2PI = 0.15915494309189535f;
    v4u xv[2];
    unsigned ep, pl, pa, pcr, pci, pd, pf;
    {
        const char* const pc = (const char*)(__builtin_amdgcn_s_getpc() & ~127ull);
        if (wave >= 8) {
            const int myrow = 32 * (b >> 2) + (b & 3) + 4 * (wave - 8);
            auto rx = mkrs(A.x, LSEQ * DM * 2);
            xv[0] = __builtin_amdgcn_raw_buffer_load_b128(rx, myrow * 2048 + lane * 16, 0, 0);
            xv[1] = __builtin_amdgcn_raw_buffer_load_b128(rx, myrow * 2048 + 1024 + lane * 16, 0, 0);
            ep = __builtin_amdgcn_raw_buffer_load_b32(rws, WS_EPOCH, 0, AUX_SC1);
        } else {
            const int ch = cg * 4 + (wave & 3);
            pl = *(const unsigned*)(A.lar + ch * 128 + 2 * lane); pa = *(const unsigned*)(A.aim + ch * 128 + 2 * lane);
            pcr = *(const unsigned*)(A.cre + ch * 128 + 2 * lane); pci = *(const unsigned*)(A.cim + ch * 128 + 2 * lane);
            pd = __builtin_amdgcn_raw_buffer_load_b32(mkrs(A.ldt, DM * 2), (ch & ~1) * 2, 0, 0);
            pf = __builtin_amdgcn_raw_buffer_load_b32(mkrs(pc, CODE_PF), tid * 128, 0, 0);
            if (wave == 0) ((unsigned*)(lds + L_CT))[lane] = 0;
        }
    }
    asm volatile("s_waitcnt lgkmcnt(0)" ::: "memory");
    __builtin_amdgcn_sched_barrier(0);
    __builtin_amdgcn_s_barrier();
    int wave2 = wave;
    asm volatile("" : "+s"(wave2));
    if (__builtin_expect(wave2 >= 8, 1)) {
        const int hw = wave - 8, T = tid - 512;
        __builtin_amdgcn_s_setprio(3);
        const int myrow = 32 * (b >> 2) + (b & 3) + 4 * hw;
        v4u wv[2];
        auto rw = mkrs(A.wr, DM * 2);
        wv[0] = __builtin_amdgcn_raw_buffer_load_b128(rw, lane * 16, 0, 0);
        wv[1] = __builtin_amdgcn_raw_buffer_load_b128(rw, 1024 + lane * 16, 0, 0);
        v2u dsk2 = __builtin_amdgcn_raw_buffer_load_b64(mkrs(A.dsk, DM * 2), cg * 8, 0, 0);
        __builtin_amdgcn_sched_barrier(0);
        asm volatile("" : "+v"(xv[0]), "+v"(xv[1]));
        STAMP(1);
        *(v4u*)(lds + L_U + hw * LNS + lane * 16) = xv[0];
        *(v4u*)(lds + L_U + hw * LNS + 1024 + lane * 16) = xv[1];
        f2 f[8];
#pragma unroll
        for (int e = 0; e < 8; e++) f[e] = (f2){bflo(xv[e >> 2][e & 3]), bfhi(xv[e >> 2][e & 3])};
        f2 s2 = ((f[0] + f[1]) + (f[2] + f[3])) + ((f[4] + f[5]) + (f[6] + f[7]));
        const float mean = wave_sum(s2.x + s2.y) * (1.0f / 1024);
        f2 q2 = {0, 0};
#pragma unroll
        for (int e = 0; e < 8; e++) { const f2 d = f[e] - mean; q2 = __builtin_elementwise_fma(d, d, q2); }
        const float var = wave_sum(q2.x + q2.y) * (1.0f / 1024);
        const float mbf = rbf(mean), sd = rbf(__builtin_amdgcn_sqrtf(rbf(rbf(var) + 1e-5f)));
        const unsigned ms = (__float_as_uint(mbf) >> 16) | (__float_as_uint(sd) & 0xffff0000u);
        if (lane == 0) *ctl(lds, C_ST + hw) = ms;
        lds_arrive(lds, C_LN, lane);
        STAMP(2);
        asm volatile("" : "+v"(ep), "+v"(dsk2), "+v"(wv[0]), "+v"(wv[1]));
        const unsigned tag = ep + 1u, tag16 = tag & 0xffffu;
        {
            const float rinv = __builtin_amdgcn_rcpf(sd);
            float acc = 0;
#pragma unroll
            for (int e = 0; e < 8; e++) {
                const f2 d = f[e] - mbf;
                const unsigned dp = pkbf(d.x, d.y);
                const f2 n = (f2){bflo(dp), bfhi(dp)} * rinv;
                acc = dot2(pkbf(n.x, n.y), wv[e >> 2][e & 3], acc);
            }
            const float r = wave_sum(acc);
            const unsigned rb = pkbf(r, r) & 0xffffu;
            const unsigned key = (rb & 0x8000u) ? (~rb & 0xffffu) : (rb | 0x8000u);
            asm volatile("s_mov_b64 exec, 1\n\tds_max_u32 %0, %1\n\ts_mov_b64 exec, -1" :: "v"(ldsb + L_CT + C_CMAX * 4), "v"((key << 16) | (unsigned)(2047 - myrow)) : "memory");
            if (lds_arrive_rtn(lds, C_CN, lane) == 7) {
                const unsigned k2 = *ctl(lds, C_CMAX);
                const unsigned mb = *ctl(lds, C_ST + (((2047 - (k2 & 0xffffu)) >> 2) & 7));
                if (lane == 0) __builtin_amdgcn_raw_buffer_store_b128((v4u){k2, tag, mb, tag}, rws, WS_CAND + b * 16, 0, AUX_SC1);
            }
        }
        STAMP(3);
        lds_wait_ge(lds, C_LN, 8);
        {
            const unsigned m = *ctl(lds, C_ST + (lane & 7));
            const unsigned w1 = (m << 16) | tag16, w3 = (m & 0xffff0000u) | tag16;
#pragma unroll
            for (int i = 0; i < 4; i++) {
                const int cgx = (T >> 3) + 64 * i;
                const v2u x4 = *(const v2u*)(lds + L_U + (lane & 7) * LNS + cgx * 8);
                __builtin_amdgcn_raw_buffer_store_b128((v4u){x4.x, w1, x4.y, w3}, rws, WS_XT + (cgx * 256 + ((b + cgx) & 255)) * 128 + (lane & 7) * 16, 0, AUX_SC1);
            }
        }
        *ctl(lds, C_TAG) = tag;
        lds_arrive(lds, C_SI, lane);
        __builtin_amdgcn_s_setprio(1);
        STAMP(4);
        const int g4s = ((g4 & 1) << 1) | (g4 >> 1);
        if (hw < 7) {
            const int v = 8 * ((hw == 3 || hw == 5) ? g4s : g4) + l15;
            const int o = (hw == 0 ? 18 : hw == 1 ? 21 : hw == 2 ? 26 : hw == 3 ? 21 : hw == 4 ? 20 : hw == 5 ? 19 : 18);
            const int w = hw == 0 ? 3 : hw == 1 ? 5 : hw == 2 ? 3 : hw == 3 ? 5 : hw == 4 ? 7 : hw == 5 ? 9 : 2;
            *(v4u*)(lds + L_BD + hw * 1024 + lane * 16) = band_frag(v - o, w, hw == 6 ? 0xffffu : 0x3C00u);
        }
        lds_arrive(lds, C_BD, lane);
#define POLL_DELAY 8
        __builtin_amdgcn_s_sleep(POLL_DELAY);
        v4u gv[4];
        {
            const int G = 8 * hw + (lane >> 3);
            _Pragma("nounroll") for (unsigned s = 0; s < SPIN_CAP; s++) {
#pragma unroll
                for (int j = 0; j < 4; j++) gv[j] = __builtin_amdgcn_raw_buffer_load_b128(rws, WS_XT + (cg * 256 + ((4 * G + j + cg) & 255)) * 128 + (lane & 7) * 16, 0, AUX_SC1);
                unsigned bad = 0;
#pragma unroll
                for (int j = 0; j < 4; j++) bad |= (gv[j].y ^ tag16) | (gv[j].w ^ tag16);
                if (__all((bad & 0xffffu) == 0)) break;
                __builtin_amdgcn_s_sleep(4);
            }
        }
        __builtin_amdgcn_s_setprio(3);
        STAMP(5);
        float base[4][4];
        {
            const float d1[4] = {(1.0f + bflo(dsk2.x)) * (1.0f / 128), (1.0f + bfhi(dsk2.x)) * (1.0f / 128), (1.0f + bflo(dsk2.y)) * (1.0f / 128), (1.0f + bfhi(dsk2.y)) * (1.0f / 128)};
            float un[4][4];
#pragma unroll
            for (int r = 0; r < 4; r++) {
                const float m = bfhi(gv[r].y), rinv = __builtin_amdgcn_rcpf(bfhi(gv[r].w)) * 128.0f;
#pragma unroll
                for (int p = 0; p < 2; p++) {
                    const unsigned xw = p ? gv[r].z : gv[r].x;
                    const f2 xx = {bflo(xw), bfhi(xw)};
                    const f2 d = xx - m;
                    const unsigned dp = pkbf(d.x, d.y);
                    const f2 n = (f2){bflo(dp), bfhi(dp)} * rinv;
                    const unsigned np = pkbf(n.x, n.y);
                    const f2 u = {bflo(np), bfhi(np)};
                    const f2 bs = __builtin_elementwise_fma(u, (f2){d1[2 * p], d1[2 * p + 1]}, xx);
                    un[r][2 * p] = u.x; un[r][2 * p + 1] = u.y; base[r][2 * p] = bs.x; base[r][2 * p + 1] = bs.y;
                }
            }
            const int off = uslot((T >> 1) + 68) * 16 + ((T & 1) ? 0 : 8);
#pragma unroll
            for (int c = 0; c < 4; c++) *(v2u*)(lds + L_U + c * UB + off) = (v2u){pkh(un[3][c], un[2][c]), pkh(un[1][c], un[0][c])};
            if (l31 < 17) *(v4u*)(lds + L_U + (T >> 7) * UB + (uslot((T >> 5) & 3) + l31) * 16) = (v4u){0, 0, 0, 0};
        }
        lds_arrive(lds, C_U, lane);
        STAMP(6);
        if (hw >= 4) {
            const int c = hw - 4;
            char* const U = lds + L_U + c * UB;
            char* const R = lds + L_XY + c * XYB;
            lds_wait_ge(lds, C_T + c, 1);
            const int e4 = 79 - l15 + 4 * g4, e8 = 127 - l15 + 4 * g4;
            const char* const f4b = R + O_FT + (e4 & 1) * 192 + (e4 >> 1) * 4;
            const char* const f8b = R + O_FT + (e8 & 1) * 192 + (e8 >> 1) * 4;
            auto ldTf = [&](const char* bp, int ka, int d) {
                const unsigned* pa_ = (const unsigned*)(bp + ka * 384 - 32 * d); const unsigned* pb_ = (const unsigned*)(bp + (ka + 1) * 384 - 32 * d);
                return (v4u){pa_[0], pa_[1], pb_[0], pb_[1]};
            };
            lds_wait_ge(lds, C_BD, 8);
            v4u bd[4];
#pragma unroll
            for (int k = 0; k < 4; k++) bd[k] = *(const v4u*)(lds + L_BD + k * 1024 + lane * 16);
            v4u t01[3], t23[3];
#pragma unroll
            for (int d = 0; d < 3; d++) { t01[d] = ldTf(f4b, 0, d + 2); t23[d] = ldTf(f4b, 2, d + 2); }
            lds_wait_ge(lds, C_U, 8);
            STAMP(7);
            const char* const uap = U + uslot(2 * l15 + 1 - g4 + 68) * 16;
            const char* const ubp = U + uslot(2 * l15 - g4s + 68) * 16;
            const f4 z4 = {0, 0, 0, 0};
            auto pkW = [&](f4 a, f4 b_) { return (v4u){pkh(a[0], a[1]), pkh(a[2], a[3]), pkh(b_[0], b_[1]), pkh(b_[2], b_[3])}; };
            f4 F[8];
#pragma unroll
            for (int t = 2; t < 8; t++) F[t] = z4;
            const int s0 = 4 * l15 + g4;
            char* const fw = R + O_YF + (s0 + (s0 >> 3)) * 16;
#pragma unroll
            for (int tp = 0; tp < 6; tp++) {
                const v4u ua = *(const v4u*)(uap + tp * 128), ub = *(const v4u*)(ubp + tp * 128);
                const v4u w01 = pkW(MFMA16(as_h8(ua), as_h8(bd[0]), z4), MFMA16(as_h8(ua), as_h8(bd[1]), z4));
                const v4u w23 = pkW(MFMA16(as_h8(ua), as_h8(bd[2]), z4), MFMA16(as_h8(ub), as_h8(bd[3]), z4));
#pragma unroll
                for (int d = 2; d <= 4; d++) if (tp + d < 8) {
                    F[tp + d] = MFMA16(as_h8(w01), as_h8(t01[d - 2]), F[tp + d]);
                    F[tp + d] = MFMA16(as_h8(w23), as_h8(t23[d - 2]), F[tp + d]);
                }
                if (tp == 1) {
                    *(f4*)(fw + 72 * 16 * 2) = F[2]; *(f4*)(fw + 72 * 16 * 3) = F[3];
                    lds_arrive(lds, C_Y0, lane);
                    __builtin_amdgcn_s_setprio(1);
                }
            }
            __builtin_amdgcn_sched_barrier(0);
            {
                v4u t45[4];
#pragma unroll
                for (int d = 0; d < 4; d++) t45[d] = ldTf(f8b, 4, d + 4);
                const v4u b4 = *(const v4u*)(lds + L_BD + 4096 + lane * 16), b5 = *(const v4u*)(lds + L_BD + 5120 + lane * 16);
                const v4u an = *(const v4u*)(lds + L_BD + 6144 + lane * 16) & *ctl(lds, C_K1024 + c);
#pragma unroll
                for (int tp = 0; tp < 4; tp++) {
                    const v4u ua = *(const v4u*)(uap + tp * 128), ub = *(const v4u*)(ubp + tp * 128);
                    const v4u w45 = pkW(MFMA16(as_h8(ua), as_h8(b4), z4), MFMA16(as_h8(ub), as_h8(b5), z4));
                    F[tp + 4] = MFMA16(as_h8(an), as_h8(ua), F[tp + 4]);
#pragma unroll
                    for (int d = 4; d <= 7; d++) if (tp + d < 8) F[tp + d] = MFMA16(as_h8(w45), as_h8(t45[d - 4]), F[tp + d]);
                }
            }
#pragma unroll
            for (int t = 4; t < 8; t++) *(f4*)(fw + 72 * 16 * t) = F[t];
            lds_arrive(lds, C_Y1, lane);
            STAMP(8);
        }
        const unsigned idx = 2047 - (lds_wait_ge(lds, C_IDX, 1) & 2047u);
        const f4 osc = *(const f4*)ctl(lds, C_OSC);
        __builtin_amdgcn_s_setprio(3);
        lds_wait_ge(lds, hw >= 4 ? C_Y1 : C_Y0, 8);
        STAMP(9);
        __builtin_amdgcn_s_setprio(0);
        {
            const char* const yp = lds + L_XY + (T + (T >> 3)) * 16;
            f4 yv[4];
#pragma unroll
            for (int c = 0; c < 4; c++) yv[c] = *(const f4*)(yp + c * XYB + O_YD);
            if (hw >= 2) {
#pragma unroll
                for (int c = 0; c < 4; c++) yv[c] += *(const f4*)(yp + c * XYB + O_YF);
            }
#pragma unroll
            for (int r = 0; r < 4; r++) {
                const int row = 4 * T + r;
                const float o0 = fmaf(yv[0][r], osc[0], base[r][0]), o1 = fmaf(yv[1][r], osc[1], base[r][1]), o2 = fmaf(yv[2][r], osc[2], base[r][2]), o3 = fmaf(yv[3][r], osc[3], base[r][3]);
                __builtin_amdgcn_raw_buffer_store_b64((v2u){pkbf(o0, o1), pkbf(o2, o3)}, rout, row == (int)idx ? 0x7ffffff0 : row * 2048 + cg * 8, 0, 0);
                if (row == (int)idx) {
                    *(f4*)ctl(lds, C_ROWV) = (f4){o0, o1, o2, o3};
                    asm volatile("s_waitcnt lgkmcnt(0)" ::: "memory");
                    __hip_atomic_store(ctl(lds, C_ROWF), 1u, __ATOMIC_RELAXED, __HIP_MEMORY_SCOPE_WORKGROUP);
                }
            }
        }
        STAMP(10);
        SWAITVM;
        STAMP(11);
        SFLUSH(A.ws);
        __builtin_amdgcn_endpgm();
    } else if (wave2 < 4) {
        __builtin_amdgcn_s_setprio(2);
        asm volatile("" : "+v"(pl), "+v"(pa), "+v"(pcr), "+v"(pci), "+v"(pd));
        const float ldt = (wave & 1) ? bfhi(pd) : bflo(pd);
        STAMP(1);
        char* const R = lds + L_XY + wave * XYB;
        {
            const float dt = rbf(fexp(ldt));
            float ar2[2], air[2];
            ar2[0] = (-rbf(fexp(bflo(pl))) * dt) * LOG2E; ar2[1] = (-rbf(fexp(bfhi(pl))) * dt) * LOG2E;
            air[0] = (bflo(pa) * dt) * INV2PI; air[1] = (bfhi(pa) * dt) * INV2PI;
            float mx = fmaxf(fabsf(bflo(pcr)) + fabsf(bflo(pci)), fabsf(bfhi(pcr)) + fabsf(bfhi(pci))) * (2.0f * dt);
            mx = wave_max(mx);
            int E = (__float_as_uint(mx) >> 23) & 0xff; E = min(max(E, 20), 240);
            const float cs = __uint_as_float((unsigned)(262 - E) << 23);
            if (lane == 0) *ctl(lds, C_OSC + wave) = (unsigned)(E - 14) << 23;
            f2 C[2] = {(f2){bflo(pcr) * dt * cs, -(bflo(pci) * dt * cs)}, (f2){bfhi(pcr) * dt * cs, -(bfhi(pci) * dt * cs)}};
            const unsigned xb = ldsb + L_XY + wave * XYB + (lane & 1) * 8;
            const int sl = lane >> 1;
            auto putX = [&](int row, const f2* a) { ldsst<v2u>(xb + ((sl ^ (row & 15)) << 4), row * 512, (v2u){pkh(a[0].x, a[0].y), pkh(a[1].x, a[1].y)}); };
            auto mul2 = [&](f2* o, const f2* a, const f2* m) { o[0] = cmul(a[0], m[0]); o[1] = cmul(a[1], m[1]); };
            f2 z[2], zb[2], z2[2], t[2], p256[2], p258[2], p260[2], p262[2], p512[2], p516[2], p772[2];
#pragma unroll
            for (int m = 0; m < 2; m++) { z[m] = cexp_p(ar2[m], -air[m], 1.0f); zb[m] = cexp_p(ar2[m], -air[m], 256.0f); z2[m] = cmul(z[m], z[m]); t[m] = C[m]; }
#pragma unroll
            for (int e = 0; e < 8; e++) { putX(e, t); if (e < 7) mul2(t, t, z); }
            mul2(p256, C, zb); mul2(p258, p256, z2); mul2(p260, p258, z2); mul2(p262, p260, z2);
            putX(8, p256); putX(9, p258); putX(10, p260); putX(11, p262);
            mul2(p512, p256, zb); mul2(p516, p260, zb); mul2(t, p512, zb); mul2(p772, p516, zb);
            putX(12, p512); putX(13, p516); putX(14, t); putX(15, p772);
#pragma unroll
            for (int e = 0; e < 5; e++) { mul2(t, t, zb); putX(16 + e, t); }
            STAMP(2);
            ygen(ar2, air, 128.0f, ldsb + L_XY + wave * XYB + 37 * 512, lane);
        }
        const int o1 = 64 + 16 * l31 + 8 * hh;
        int ad[4];
        {
            const int bo = l31 & 1;
            int a0, a1, st2;
            if (hh) { a0 = (bo ? 1 : 3) * 384 + 2 * (79 - (31 + ((l31 + 1) >> 1))); a1 = (bo ? 2 : 0) * 384 + 2 * (79 - (32 + (l31 >> 1))); st2 = 32; }
            else { a0 = (bo ? 4 : 5) * 384 + 2 * (127 - (63 + ((l31 + 1) >> 1))); a1 = a0 - 32; st2 = 64; }
            ad[0] = a0; ad[1] = a1; ad[2] = a0 - st2; ad[3] = a1 - st2;
            if (lane == 0) ad[0] = 3 * 384 + 2 * 16;
            if (lane == 32) ad[0] = 3 * 384 + 2 * 70;
#pragma unroll
            for (int e = 0; e < 4; e++) ad[e] = opq(ad[e] + ldsb + L_XY + wave * XYB + O_FT);
        }
        STAMP(3);
        lds_wait_ge(lds, C_X + wave, 1);
        STAMP(4);
        f16v kacc;
#pragma unroll
        for (int e = 0; e < 16; e++) kacc[e] = 0.0f;
        {
            const int lp = hh ^ (l31 & 15);
            v4u xa[16], yb[16];
#pragma unroll
            for (int t = 0; t < 16; t++) {
                const int so = ((lp ^ ((2 * t) & 15)) + ((2 * t) & 16)) << 4;
                xa[t] = *(const v4u*)(R + l31 * 512 + so);
                yb[t] = *(const v4u*)(R + (21 + l31) * 512 + so);
                if (t >= 4) kacc = MFMA32(as_h8(xa[t - 4]), as_h8(yb[t - 4]), kacc);
                __builtin_amdgcn_sched_barrier(0);
            }
            const v4u z = {0, 0, 0, 0};
            *(v4u*)(R + O_FT + lane * 16) = z; *(v4u*)(R + O_FT + 1024 + lane * 16) = z; if (lane < 16) *(v4u*)(R + O_FT + 2048 + lane * 16) = z;
            { const int cp = lane >> 4, o = lane & 15; *(v4u*)(R + O_KD + cp * 1344 + (o < 5 ? o * 16 : 1072 + (o - 5) * 16)) = z; }
#pragma unroll
            for (int t = 12; t < 16; t++) kacc = MFMA32(as_h8(xa[t]), as_h8(yb[t]), kacc);
        }
        STAMP(5);
        {
            char* const KD = R + O_KD;
            char* const FT = R + O_FT;
            float kv[12];
#pragma unroll
            for (int e = 0; e < 12; e++) kv[e] = kacc[e];
            const f2 sw = swap32(kv[0], kv[0]);
            float n0 = hh ? dppf<0x130>(sw.x) : sw.y;
            const float k256 = __uint_as_float(__builtin_amdgcn_readlane(__float_as_uint(kv[4]), 0));
            n0 = lane == 63 ? k256 : n0;
            const unsigned p01 = pkh(kv[0], kv[1]), p23 = pkh(kv[2], kv[3]), p12 = pkh(kv[1], kv[2]), p3n = pkh(kv[3], n0);
            *(v2u*)(KD + o1) = (v2u){p01, p23};
            *(v2u*)(KD + KD1 + o1) = (v2u){p12, p3n};
            *(unsigned*)(KD + KD2 + o1 - 4) = p01; *(unsigned*)(KD + KD2 + o1) = p23;
            *(unsigned*)(KD + KD3 + o1 - 4) = p12; *(unsigned*)(KD + KD3 + o1) = p3n;
            const float vn = dppf<0x130>(kv[4]);
            const unsigned q00 = pkh(kv[4], kv[4]), q22 = pkh(kv[6], kv[6]);
            if (hh == 0) {
                const v4u ev = {q00, pkh(kv[5], kv[6]), q22, pkh(kv[7], vn)};
                const v4u od = {pkh(kv[4], kv[5]), q22, pkh(kv[6], kv[7]), pkh(vn, vn)};
                const int o2 = 576 + 16 * l31;
                *(v4u*)(KD + o2) = ev;
                *(v2u*)(KD + KD1 + o2) = (v2u){od[0], od[1]}; *(v2u*)(KD + KD1 + o2 + 8) = (v2u){od[2], od[3]};
                unsigned* const e2 = (unsigned*)(KD + KD2 + o2 - 4); unsigned* const o3 = (unsigned*)(KD + KD3 + o2 - 4);
#pragma unroll
                for (int k = 0; k < 4; k++) { e2[k] = ev[k]; o3[k] = od[k]; }
            }
            if (lane == 0) { *(u16*)(KD + KD1 + 62) = (u16)p01; *(u16*)(KD + KD3 + 58) = (u16)p01; *ctl(lds, C_K1024 + wave) = pkh(kv[8], kv[8]); }
            if (lane == 32) {
                const unsigned q0z = q00 & 0xffffu;
                *(v2u*)(KD + 1088) = (v2u){q00, q0z};
                *(unsigned*)(KD + KD2 + 1084) = q00; *(unsigned*)(KD + KD2 + 1088) = q0z;
                *(unsigned*)(KD + KD1 + 1088) = q00; *(unsigned*)(KD + KD3 + 1084) = q00;
                *(u16*)(FT + 5 * 384) = (u16)pkh(kv[8], kv[8]);
            }
#pragma unroll
            for (int e = 0; e < 4; e++) {
                const u16 hv = (u16)pkh(hh ? kv[4 + e] : kv[8 + e], 0.0f);
                ldsst<u16>(ad[e], 0, hv); ldsst<u16>(ad[e], 190, hv);
            }
        }
        lds_arrive(lds, C_T + wave, lane);
        STAMP(6);
        const int rR = 16 * ((l31 >> 2) & 1) + 4 * (l31 >> 3) + (l31 & 3);
        const int tau = rR + 8 * hh + 1;
        const unsigned ka0 = opq(ldsb + L_XY + wave * XYB + O_KD + (tau & 3) * 1344 + ((tau & 3) ? 8 : 0) + (tau >> 2) * 8);
        const unsigned ka1 = opq(ka0 + 8);
        unsigned ub = ldsb + L_U + wave * UB + (l31 + 96 - 96 * hh) * 16;
        unsigned yw = ldsb + L_XY + wave * XYB + O_YD + (9 * l31 + 4 * hh) * 16;
        constexpr int PD = 4;
        v2u kf0[35], kf1[35];
#pragma unroll
        for (int j = 0; j < PD; j++) { kf0[j] = ldsld<v2u>(ka0, 32 * j); kf1[j] = ldsld<v2u>(ka1, 32 * j); }
        lds_wait_ge(lds, C_U, 8);
        __builtin_amdgcn_s_setprio(2);
        STAMP(7);
#pragma unroll 1
        for (int be = 0; be < 2; be++) {
            auto uoff = [](int j) { const int t = 3 - 2 * j; const int fl = (t >= 0) ? (t >> 2) : -((-t + 3) >> 2); return (uplane(t & 3) - 96 + 17 + fl) * 16; };
            v4u uf[35];
#pragma unroll
            for (int j = 0; j < PD; j++) uf[j] = ldsld<v4u>(ub, uoff(j));
            f16v acc;
#pragma unroll
            for (int e = 0; e < 16; e++) acc[e] = 0.0f;
            __builtin_amdgcn_sched_barrier(0);
#pragma unroll
            for (int j = 0; j < 35; j++) {
                acc = MFMA32(as_h8((v4u){kf0[j].x, kf0[j].y, kf1[j].x, kf1[j].y}), as_h8(uf[j]), acc);
                const int jn = (j + PD) % 35;
                kf0[jn] = ldsld<v2u>(ka0, 32 * jn); kf1[jn] = ldsld<v2u>(ka1, 32 * jn);
                if (j + PD < 35) uf[j + PD] = ldsld<v4u>(ub, uoff(j + PD));
                __builtin_amdgcn_sched_barrier(0);
            }
#pragma unroll
            for (int g = 0; g < 4; g++) ldsst<f4>(yw, g * 16, (f4){acc[4 * g], acc[4 * g + 1], acc[4 * g + 2], acc[4 * g + 3]});
            lds_arrive(lds, C_Y0 + be, lane);
            __builtin_amdgcn_s_setprio(1);
            ub += 32 * 16; yw += 288 * 16;
            if (be == 0) STAMP(8); else STAMP(9);
        }
        asm volatile("" :: "v"(pf));
        SFLUSH(A.ws);
        __builtin_amdgcn_endpgm();
    } else {
        const int wv = wave - 4;
        const bool moe = (b >> 3) < 28;
        const int mo = (b & 7) * 28 + (b >> 3), fb = mo >> 2, kq = mo & 3;
        const int k0 = kq * 256 + wv * 64 + (lane >> 3) * 8;
        v4u wgv[8], wuv[8];
        auto rg = mkrs(A.wg, moe ? DM * FF * 2 : 0), ru = mkrs(A.wu, moe ? DM * FF * 2 : 0);
#define W1N 4
#pragma unroll
        for (int i = 0; i < W1N; i++) {
            wgv[i] = __builtin_amdgcn_raw_buffer_load_b128(rg, (k0 + i) * 7168 + fb * 128 + (lane & 7) * 16, 0, AUX_W);
            wuv[i] = __builtin_amdgcn_raw_buffer_load_b128(ru, (k0 + i) * 7168 + fb * 128 + (lane & 7) * 16, 0, AUX_W);
        }
        __builtin_amdgcn_sched_barrier(0);
        asm volatile("" : "+v"(pl), "+v"(pa), "+v"(pd), "+v"(pf));
        __builtin_amdgcn_s_setprio(2);
        STAMP(1);
        {
            const float dt = rbf(fexp((wave & 1) ? bfhi(pd) : bflo(pd)));
            float ar2[2], air[2];
            ar2[0] = (-rbf(fexp(bflo(pl))) * dt) * LOG2E; ar2[1] = (-rbf(fexp(bfhi(pl))) * dt) * LOG2E;
            air[0] = (bflo(pa) * dt) * INV2PI; air[1] = (bfhi(pa) * dt) * INV2PI;
            ygen(ar2, air, 0.0f, ldsb + L_XY + wv * XYB + 21 * 512, lane);
        }
        lds_arrive(lds, C_X + wv, lane);
        __builtin_amdgcn_s_setprio(0);
        STAMP(2);
        if (!moe && wv != 0) { SFLUSH(A.ws); __builtin_amdgcn_endpgm(); }
        lds_wait_ge(lds, C_SI, 8);
        const unsigned tag = *ctl(lds, C_TAG);
#pragma unroll
        for (int i = W1N; i < 8; i++) {
            wgv[i] = __builtin_amdgcn_raw_buffer_load_b128(rg, (k0 + i) * 7168 + fb * 128 + (lane & 7) * 16, 0, AUX_W);
            wuv[i] = __builtin_amdgcn_raw_buffer_load_b128(ru, (k0 + i) * 7168 + fb * 128 + (lane & 7) * 16, 0, AUX_W);
        }
        __builtin_amdgcn_sched_barrier(0);
        if (wv == 0) {
            v4u cn[4];
            _Pragma("nounroll") for (unsigned s = 0; s < SPIN_CAP; s++) {
#pragma unroll
                for (int j = 0; j < 4; j++) cn[j] = __builtin_amdgcn_raw_buffer_load_b128(rws, WS_CAND + (lane + 64 * j) * 16, 0, AUX_SC1);
                unsigned bad = 0;
#pragma unroll
                for (int j = 0; j < 4; j++) bad |= (cn[j].y ^ tag) | (cn[j].w ^ tag);
                if (__all(bad == 0)) break;
                __builtin_amdgcn_s_sleep(2);
            }
            __builtin_amdgcn_s_setprio(3);
            const unsigned mk = max(max(cn[0].x, cn[1].x), max(cn[2].x, cn[3].x));
            const unsigned bst = wave_umax(mk);
            if (mk == bst) {
                *ctl(lds, C_MS) = cn[0].x == bst ? cn[0].z : cn[1].x == bst ? cn[1].z : cn[2].x == bst ? cn[2].z : cn[3].z;
                *ctl(lds, C_IDX) = bst | 0x8000u;
                if (b == 0) __builtin_amdgcn_raw_buffer_store_b32(tag, rws, WS_EPOCH, 0, AUX_SC1);
            }
        }
        const unsigned best = lds_wait_ge(lds, C_IDX, 1);
        __builtin_amdgcn_s_setprio(3);
        STAMP(3);
        const unsigned idx = 2047 - (best & 2047u);
        const unsigned key = best >> 16;
        const float rsel = bflo((key & 0x8000u) ? (key & 0x7fffu) : (~key & 0xffffu));
        bool fin = true;
        if (moe) {
            auto rx = mkrs(A.x, LSEQ * DM * 2);
            const v4u xs = __builtin_amdgcn_raw_buffer_load_b128(rx, idx * 2048 + k0 * 2, 0, 0);
            v2u wdv[16];
            auto rd = mkrs(A.wd, DM * FF * 2);
#pragma unroll
            for (int i = 0; i < WD1; i++) wdv[i] = __builtin_amdgcn_raw_buffer_load_b64(rd, (fb * 64 + wv * 16 + i) * 2048 + kq * 512 + lane * 8, 0, AUX_W);
            __builtin_amdgcn_sched_barrier(0);
            const unsigned ms = *ctl(lds, C_MS);
            const float m = bflo(ms), rinv = __builtin_amdgcn_rcpf(bfhi(ms));
            float acc[16];
#pragma unroll
            for (int c = 0; c < 16; c++) acc[c] = 0;
#pragma unroll
            for (int i2 = 0; i2 < 4; i2++) {
                const unsigned dp = pkbf(bflo(xs[i2]) - m, bfhi(xs[i2]) - m);
                const unsigned np = pkbf(bflo(dp) * rinv, bfhi(dp) * rinv);
                const unsigned nlo = np & 0xffffu, nlo1 = np << 16, nhi = np >> 16, nhi1 = np & 0xffff0000u;
#pragma unroll
                for (int s = 0; s < 2; s++) {
                    const int i = 2 * i2 + s;
                    const unsigned xl = s ? nhi : nlo, xh = s ? nhi1 : nlo1;
#pragma unroll
                    for (int c = 0; c < 4; c++) {
                        acc[2 * c] = dot2(wgv[i][c], xl, acc[2 * c]);
                        acc[2 * c + 1] = dot2(wgv[i][c], xh, acc[2 * c + 1]);
                        acc[8 + 2 * c] = dot2(wuv[i][c], xl, acc[8 + 2 * c]);
                        acc[8 + 2 * c + 1] = dot2(wuv[i][c], xh, acc[8 + 2 * c + 1]);
                    }
                }
            }
            __builtin_amdgcn_sched_barrier(0);
#pragma unroll
            for (int i = WD1; i < 16; i++) wdv[i] = __builtin_amdgcn_raw_buffer_load_b64(rd, (fb * 64 + wv * 16 + i) * 2048 + kq * 512 + lane * 8, 0, AUX_W);
            __builtin_amdgcn_sched_barrier(0);
            STAMP(4);
            float r8[8], r4[4], r2[2];
#pragma unroll
            for (int c = 0; c < 8; c++) { f2 t = swap32(acc[c], acc[8 + c]); r8[c] = t.x + t.y; }
#pragma unroll
            for (int c = 0; c < 4; c++) { f2 t = swap16(r8[c], r8[4 + c]); r4[c] = t.x + t.y; }
            const bool b3 = lane & 8;
#pragma unroll
            for (int c = 0; c < 2; c++) { const float keep = b3 ? r4[2 + c] : r4[c], give = b3 ? r4[c] : r4[2 + c]; r2[c] = keep + dppf<0x128>(give); }
            const int vi = (lane >> 5) * 64 + 8 * (lane & 7) + 4 * ((lane >> 4) & 1) + 2 * ((lane >> 3) & 1);
            *(f2*)(lds + L_GUR + wv * 512 + vi * 4) = (f2){r2[0], r2[1]};
            if (lds_arrive_rtn(lds, C_GU, lane) == 3) {
                const f2 s0 = *(const f2*)(lds + L_GUR + lane * 8), s1 = *(const f2*)(lds + L_GUR + 512 + lane * 8), s2 = *(const f2*)(lds + L_GUR + 1024 + lane * 8), s3 = *(const f2*)(lds + L_GUR + 1536 + lane * 8);
                const f2 s = (s0 + s1) + (s2 + s3);
                __builtin_amdgcn_raw_buffer_store_b128((v4u){__float_as_uint(s.x), tag, __float_as_uint(s.y), tag}, rws, WS_GU + mo * 1024 + lane * 16, 0, AUX_SC1);
            }
            STAMP(5);
            v2u gq, uq;
            _Pragma("nounroll") for (unsigned s = 0; s < SPIN_CAP; s++) {
                gq = __builtin_amdgcn_raw_buffer_load_b64(rws, WS_GU + (fb * 4 + g4) * 1024 + (16 * wv + l15) * 8, 0, AUX_SC1);
                uq = __builtin_amdgcn_raw_buffer_load_b64(rws, WS_GU + (fb * 4 + g4) * 1024 + 512 + (16 * wv + l15) * 8, 0, AUX_SC1);
                if (__all(((gq.y ^ tag) | (uq.y ^ tag)) == 0)) break;
                __builtin_amdgcn_s_sleep(2);
            }
            STAMP(6);
            const float gt = rbf(sum32(sum16(__uint_as_float(gq.x)))), ut = rbf(sum32(sum16(__uint_as_float(uq.x))));
            const float hv = rbf(rbf(gt * __builtin_amdgcn_rcpf(1.0f + fexp(-gt))) * ut);
            const unsigned hb = __float_as_uint(hv);
            float p4[4] = {0, 0, 0, 0};
#pragma unroll
            for (int i = 0; i < 16; i++) {
                const unsigned hs = __builtin_amdgcn_readlane(hb, i);
                const unsigned hh1 = hs, hl = hs >> 16;
                p4[0] = dot2(wdv[i].x, hl, p4[0]);
                p4[1] = dot2(wdv[i].x, hh1, p4[1]);
                p4[2] = dot2(wdv[i].y, hl, p4[2]);
                p4[3] = dot2(wdv[i].y, hh1, p4[3]);
            }
            *(f4*)(lds + L_PR + wv * 1024 + lane * 16) = (f4){p4[0], p4[1], p4[2], p4[3]};
            fin = false;
            if (lds_arrive_rtn(lds, C_P, lane) == 3) {
                const f4 s0 = *(const f4*)(lds + L_PR + lane * 16), s1 = *(const f4*)(lds + L_PR + 1024 + lane * 16), s2 = *(const f4*)(lds + L_PR + 2048 + lane * 16), s3 = *(const f4*)(lds + L_PR + 3072 + lane * 16);
                const f4 s = (s0 + s1) + (s2 + s3);
                __builtin_amdgcn_raw_buffer_store_b128((v4u){__float_as_uint(s[0]), tag, __float_as_uint(s[1]), tag}, rws, WS_P + (kq * 64 + lane) * 2048 + fb * 32, 0, AUX_SC1);
                __builtin_amdgcn_raw_buffer_store_b128((v4u){__float_as_uint(s[2]), tag, __float_as_uint(s[3]), tag}, rws, WS_P + (kq * 64 + lane) * 2048 + fb * 32 + 16, 0, AUX_SC1);
                fin = true;
            }
            STAMP(7);
        }
        if (fin) {
            v4u p0, p1;
            const bool on = lane < 48;
            _Pragma("nounroll") for (unsigned s = 0; s < SPIN_CAP; s++) {
                p0 = __builtin_amdgcn_raw_buffer_load_b128(rws, WS_P + cg * 2048 + lane * 16, 0, AUX_SC1);
                p1 = __builtin_amdgcn_raw_buffer_load_b128(rws, WS_P + cg * 2048 + (on ? 1024 : 0) + lane * 16, 0, AUX_SC1);
                if (__all(((p0.y ^ tag) | (p0.w ^ tag) | (p1.y ^ tag) | (p1.w ^ tag)) == 0)) break;
                __builtin_amdgcn_s_sleep(H3_SLEEP);
            }
            STAMP(8);
            float ya = __uint_as_float(p0.x) + (on ? __uint_as_float(p1.x) : 0.0f), yb = __uint_as_float(p0.z) + (on ? __uint_as_float(p1.z) : 0.0f);
            ya += dppf<0x122>(ya); ya += dppf<0x124>(ya); ya += dppf<0x128>(ya); ya = sum32(sum16(ya));
            yb += dppf<0x122>(yb); yb += dppf<0x124>(yb); yb += dppf<0x128>(yb); yb = sum32(sum16(yb));
            const float y0 = ya, y1 = yb, y2 = dppf<0x130>(ya), y3 = dppf<0x130>(yb);
            const float rs = rbf(__builtin_amdgcn_rcpf(1.0f + fexp(-rsel)));
            lds_wait_ge(lds, C_ROWF, 1);
            const f4 rv = *(const f4*)ctl(lds, C_ROWV);
            const float o0 = rv[0] + rbf(rbf(y0) * rs), o1 = rv[1] + rbf(rbf(y1) * rs), o2 = rv[2] + rbf(rbf(y2) * rs), o3 = rv[3] + rbf(rbf(y3) * rs);
            if (lane == 0) __builtin_amdgcn_raw_buffer_store_b64((v2u){pkbf(o0, o1), pkbf(o2, o3)}, rout, idx * 2048 + cg * 8, 0, 0);
            STAMP(9);
        }
        SFLUSH(A.ws);
    }
}

extern "C" void kernel_launch(void* const* d_in, const int* in_sizes, int n_in,
                              void* d_out, int out_size, void* d_ws, size_t ws_size,
                              hipStream_t stream) {
    KArgs a;
    a.x = (const u16*)d_in[0]; a.lar = (const u16*)d_in[1]; a.aim = (const u16*)d_in[2]; a.cre = (const u16*)d_in[3]; a.cim = (const u16*)d_in[4];
    a.ldt = (const u16*)d_in[5]; a.dsk = (const u16*)d_in[6]; a.wr = (const u16*)d_in[8];
    a.wg = (const u16*)d_in[9]; a.wu = (const u16*)d_in[10]; a.wd = (const u16*)d_in[11];
    a.out = (u16*)d_out; a.ws = (char*)d_ws;
    k_main<<<256, 1024, 0, stream>>>(a);
}
```

```cpp
#include <hip/hip_runtime.h>
#include <stdint.h>

typedef unsigned short u16;
typedef unsigned v4u __attribute__((ext_vector_type(4)));
typedef unsigned v2u __attribute__((ext_vector_type(2)));
typedef float f4 __attribute__((ext_vector_type(4)));
typedef float f2 __attribute__((ext_vector_type(2)));
typedef float f16v __attribute__((ext_vector_type(16)));
typedef _Float16 h8 __attribute__((ext_vector_type(8)));
typedef _Float16 h2 __attribute__((ext_vector_type(2)));
typedef __bf16 b2 __attribute__((ext_vector_type(2)));
#define DI __device__ __forceinline__
#define LAS __attribute__((address_space(3)))
#define AUX_NT 2
#define AUX_SC1 16
#define AUX_W 2

constexpr int LSEQ = 2048, DM = 1024, FF = 3584;
constexpr unsigned WS_EPOCH = 0;
constexpr unsigned WS_CAND = 4096;
constexpr unsigned WS_GU = 16384;
constexpr unsigned WS_P = WS_GU + 224 * 1024;
constexpr unsigned WS_XT = 1u << 20;
constexpr unsigned WS_BYTES = WS_XT + (8u << 20);
#define WD1 8
#define H3_SLEEP 8
#define CODE_PF 19456
constexpr unsigned SPIN_CAP = 1u << 16;

DI float bflo(unsigned v) { return __uint_as_float(v << 16); }
DI float bfhi(unsigned v) { return __uint_as_float(v & 0xffff0000u); }
DI unsigned pkbf(float a, float b) { b2 v = {(__bf16)a, (__bf16)b}; return __builtin_bit_cast(unsigned, v); }
DI b2 asb2(unsigned v) { return __builtin_bit_cast(b2, v); }
DI float dot2(unsigned a, unsigned b, float c) { return __builtin_amdgcn_fdot2_f32_bf16(asb2(a), asb2(b), c, false); }
DI float rbf(float a) { return bflo(pkbf(a, a)); }
DI unsigned pkh(float a, float b) { h2 v = {(_Float16)a, (_Float16)b}; return __builtin_bit_cast(unsigned, v); }
template <int C> DI float dppf(float x) { return __int_as_float(__builtin_amdgcn_update_dpp(0, __float_as_int(x), C, 0xf, 0xf, true)); }
template <int C> DI unsigned dppu(unsigned x) { return (unsigned)__builtin_amdgcn_update_dpp(0, (int)x, C, 0xf, 0xf, true); }
DI f2 swap16(float a, float b) { auto r = __builtin_amdgcn_permlane16_swap(__float_as_uint(a), __float_as_uint(b), false, false); return (f2){__uint_as_float(r[0]), __uint_as_float(r[1])}; }
DI f2 swap32(float a, float b) { auto r = __builtin_amdgcn_permlane32_swap(__float_as_uint(a), __float_as_uint(b), false, false); return (f2){__uint_as_float(r[0]), __uint_as_float(r[1])}; }
DI float sum16(float a) { f2 r = swap16(a, a); return r.x + r.y; }
DI float sum32(float a) { f2 r = swap32(a, a); return r.x + r.y; }
DI float wave_sum(float v) { v += dppf<0x121>(v); v += dppf<0x122>(v); v += dppf<0x124>(v); v += dppf<0x128>(v); return sum32(sum16(v)); }
DI float wave_max(float v) {
    v = fmaxf(v, dppf<0x121>(v)); v = fmaxf(v, dppf<0x122>(v)); v = fmaxf(v, dppf<0x124>(v)); v = fmaxf(v, dppf<0x128>(v));
    f2 r = swap16(v, v); v = fmaxf(r.x, r.y); r = swap32(v, v); return fmaxf(r.x, r.y);
}
DI unsigned wave_umax(unsigned v) {
    v = max(v, dppu<0x121>(v)); v = max(v, dppu<0x122>(v)); v = max(v, dppu<0x124>(v)); v = max(v, dppu<0x128>(v));
    auto r = __builtin_amdgcn_permlane16_swap(v, v, false, false); v = max(r[0], r[1]);
    r = __builtin_amdgcn_permlane32_swap(v, v, false, false); return max(r[0], r[1]);
}
DI float fexp(float x) {
    const float t = x * 1.4426950408889634f;
    const float lo = fmaf(x, 1.4426950408889634f, -t) + x * 1.925963033500011e-8f;
    const float e = __builtin_amdgcn_exp2f(t);
    return fmaf(e, lo * 0.6931471805599453f, e);
}
DI __amdgpu_buffer_rsrc_t mkrs(const void* p, unsigned bytes) { return __builtin_amdgcn_make_buffer_rsrc((void*)p, 0, bytes, 0x00020000); }

#define SDECL
#define STAMP(i)
#define SFLUSH(wsp)
#define SWAITVM

constexpr int UB = 6144;
constexpr int L_U = 0;
constexpr int LNS = 2080;
DI constexpr int uplane(int p) { return p == 0 ? 0 : p == 1 ? 96 : p == 2 ? 200 : 296; }
DI int uslot(int q) { return ((q & 1) ? 96 : 0) + ((q & 2) ? 200 : 0) + (q >> 2); }
constexpr int XYB = 53 * 512;
constexpr int L_XY = 4 * UB;
constexpr int O_YD = 0, O_YF = 9216, O_KD = 18432, KD1 = 1352, KD2 = 2696, KD3 = 4040, O_FT = O_KD + 5376;
static_assert(O_FT + 2304 <= XYB, "region");
constexpr int L_GUR = L_XY + 4 * XYB;
constexpr int L_PR = L_GUR + 2048;
constexpr int L_BD = L_PR + 4096;
constexpr int L_CT = L_BD + 7168;
constexpr int L_TOT = L_CT + 256;
enum { C_LN = 0, C_CMAX = 1, C_CN = 2, C_U = 3, C_Y0 = 4, C_Y1 = 5, C_GU = 6, C_P = 7, C_IDX = 8, C_ROWF = 9, C_TAG = 10, C_SI = 11, C_X = 12, C_T = 16, C_K1024 = 20, C_OSC = 24, C_ROWV = 28, C_ST = 32, C_MS = 40, C_BD = 41 };

struct KArgs {
    const u16* x; const u16* lar; const u16* aim; const u16* cre; const u16* cim; const u16* ldt; const u16* dsk; const u16* wr;
    const u16* wg; const u16* wu; const u16* wd; u16* out; char* ws;
};

DI unsigned* ctl(char* lds, int w) { return (unsigned*)(lds + L_CT) + w; }
DI void lds_arrive(char* lds, int word, int lane) {
    asm volatile("s_mov_b64 exec, 1\n\tds_add_u32 %0, %1\n\ts_mov_b64 exec, -1" :: "v"((unsigned)(size_t)(LAS unsigned*)ctl(lds, word)), "v"(1u) : "memory");
}
DI unsigned lds_arrive_rtn(char* lds, int word, int lane) {
    unsigned r;
    asm volatile("s_mov_b64 exec, 1\n\tds_add_rtn_u32 %0, %1, %2\n\ts_mov_b64 exec, -1\n\ts_waitcnt lgkmcnt(0)" : "=&v"(r) : "v"((unsigned)(size_t)(LAS unsigned*)ctl(lds, word)), "v"(1u) : "memory");
    return __builtin_amdgcn_readfirstlane(r);
}
DI unsigned lds_wait_ge(char* lds, int word, unsigned want) {
    unsigned v;
    _Pragma("nounroll") for (unsigned s = 0; s < (1u << 22); s++) {
        v = __builtin_amdgcn_readfirstlane(__hip_atomic_load(ctl(lds, word), __ATOMIC_RELAXED, __HIP_MEMORY_SCOPE_WORKGROUP));
        if (v >= want) break;
        __builtin_amdgcn_s_sleep(1);
    }
    asm volatile("" ::: "memory");
    return v;
}
DI f2 cmul(f2 a, f2 b) { f2 t = a * (f2){b.x, b.x}; return __builtin_elementwise_fma((f2){-a.y, a.x}, (f2){b.y, b.y}, t); }
DI f2 cexp_p(float ar2, float air, float p) {
    const float m = __builtin_amdgcn_exp2f(ar2 * p);
    const float t = __builtin_amdgcn_fractf(air * p);
    return (f2){m * __builtin_amdgcn_cosf(t), m * __builtin_amdgcn_sinf(t)};
}
DI h8 as_h8(v4u v) { return __builtin_bit_cast(h8, v); }
DI v4u band_frag(int v0, int width, unsigned one) {
    const unsigned long long m = ((1ull << width) - 1) << 32;
    const unsigned m8 = (unsigned)(m >> (v0 + 32));
    v4u r;
#pragma unroll
    for (int k = 0; k < 4; k++) { const unsigned t = m8 >> (2 * k); r[k] = ((t & 1u) | ((t & 2u) << 15)) * one; }
    return r;
}
DI unsigned opq(unsigned v) { asm volatile("" : "+v"(v)); return v; }
template <typename T> DI T ldsld(unsigned a, int off) { return *(const LAS T*)(size_t)(a + off); }
template <typename T> DI void ldsst(unsigned a, int off, T v) { *(LAS T*)(size_t)(a + off) = v; }
#define MFMA32(a, b, c) __builtin_amdgcn_mfma_f32_32x32x16_f16((a), (b), (c), 0, 0, 0)
#define MFMA16(a, b, c) __builtin_amdgcn_mfma_f32_16x16x32_f16((a), (b), (c), 0, 0, 0)

DI void ygen(const float* ar2, const float* air, float p0, unsigned rows, int lane) {
    f2 w[2], y[2][2];
#pragma unroll
    for (int m = 0; m < 2; m++) { w[m] = cexp_p(ar2[m], air[m], 8.0f); y[0][m] = cexp_p(ar2[m], air[m], p0); y[1][m] = cexp_p(ar2[m], air[m], p0 + 64.0f); }
    const unsigned yb = rows + (lane & 1) * 8;
    const int sl = lane >> 1;
#pragma unroll 2
    for (int s = 0; s < 8; s++) {
        const unsigned a0 = yb + s * 512 + ((sl ^ s) << 4), a1 = a0 ^ 128;
        ldsst<v2u>(a0, 0, (v2u){pkh(y[0][0].x, y[0][0].y), pkh(y[0][1].x, y[0][1].y)});
        ldsst<v2u>(a1, 4096, (v2u){pkh(y[1][0].x, y[1][0].y), pkh(y[1][1].x, y[1][1].y)});
#pragma unroll
        for (int c = 0; c < 2; c++) { y[c][0] = cmul(y[c][0], w[0]); y[c][1] = cmul(y[c][1], w[1]); }
    }
}

__global__ void __launch_bounds__(1024, 4) k_main(KArgs A) {
    __shared__ __attribute__((aligned(16))) char lds[L_TOT];
    const int b = blockIdx.x, tid = threadIdx.x, lane = tid & 63;
    const int wave = __builtin_amdgcn_readfirstlane(tid >> 6);
    const int cg = (b & 7) * 32 + (b >> 3);
    const unsigned ldsb = (unsigned)(size_t)(LAS char*)lds;
    SDECL;
    const unsigned long long tstart = __builtin_amdgcn_s_memrealtime();
    auto rws = mkrs(A.ws, WS_BYTES);
    auto rout = mkrs(A.out, LSEQ * DM * 2);
    const int l31 = lane & 31, hh = lane >> 5, l15 = lane & 15, g4 = lane >> 4;
    const float LOG2E = 1.4426950408889634f, INV2PI = 0.15915494309189535f;
    v4u xv[2];
    unsigned ep, pl, pa, pcr, pci, pd, pf;
    {
        const char* const pc = (const char*)(__builtin_amdgcn_s_getpc() & ~127ull);
        if (wave >= 8) {
            const int myrow = 32 * (b >> 2) + (b & 3) + 4 * (wave - 8);
            auto rx = mkrs(A.x, LSEQ * DM * 2);
            xv[0] = __builtin_amdgcn_raw_buffer_load_b128(rx, myrow * 2048 + lane * 16, 0, 0);
            xv[1] = __builtin_amdgcn_raw_buffer_load_b128(rx, myrow * 2048 + 1024 + lane * 16, 0, 0);
            ep = __builtin_amdgcn_raw_buffer_load_b32(rws, WS_EPOCH, 0, AUX_SC1);
        } else {
            const int ch = cg * 4 + (wave & 3);
            pl = *(const unsigned*)(A.lar + ch * 128 + 2 * lane); pa = *(const unsigned*)(A.aim + ch * 128 + 2 * lane);
            pcr = *(const unsigned*)(A.cre + ch * 128 + 2 * lane); pci = *(const unsigned*)(A.cim + ch * 128 + 2 * lane);
            pd = __builtin_amdgcn_raw_buffer_load_b32(mkrs(A.ldt, DM * 2), (ch & ~1) * 2, 0, 0);
            pf = __builtin_amdgcn_raw_buffer_load_b32(mkrs(pc, CODE_PF), tid * 128, 0, 0);
            if (wave == 0) ((unsigned*)(lds + L_CT))[lane] = 0;
        }
    }
    asm volatile("s_waitcnt lgkmcnt(0)" ::: "memory");
    __builtin_amdgcn_sched_barrier(0);
    __builtin_amdgcn_s_barrier();
    int wave2 = wave;
    asm volatile("" : "+s"(wave2));
    if (__builtin_expect(wave2 >= 8, 1)) {
        const int hw = wave - 8, T = tid - 512;
        __builtin_amdgcn_s_setprio(3);
        const int myrow = 32 * (b >> 2) + (b & 3) + 4 * hw;
        v4u wv[2];
        auto rw = mkrs(A.wr, DM * 2);
        wv[0] = __builtin_amdgcn_raw_buffer_load_b128(rw, lane * 16, 0, 0);
        wv[1] = __builtin_amdgcn_raw_buffer_load_b128(rw, 1024 + lane * 16, 0, 0);
        v2u dsk2 = __builtin_amdgcn_raw_buffer_load_b64(mkrs(A.dsk, DM * 2), cg * 8, 0, 0);
        __builtin_amdgcn_sched_barrier(0);
        asm volatile("" : "+v"(xv[0]), "+v"(xv[1]));
        STAMP(1);
        *(v4u*)(lds + L_U + hw * LNS + lane * 16) = xv[0];
        *(v4u*)(lds + L_U + hw * LNS + 1024 + lane * 16) = xv[1];
        f2 f[8];
#pragma unroll
        for (int e = 0; e < 8; e++) f[e] = (f2){bflo(xv[e >> 2][e & 3]), bfhi(xv[e >> 2][e & 3])};
        f2 s2 = ((f[0] + f[1]) + (f[2] + f[3])) + ((f[4] + f[5]) + (f[6] + f[7]));
        const float mean = wave_sum(s2.x + s2.y) * (1.0f / 1024);
        f2 q2 = {0, 0};
#pragma unroll
        for (int e = 0; e < 8; e++) { const f2 d = f[e] - mean; q2 = __builtin_elementwise_fma(d, d, q2); }
        const float var = wave_sum(q2.x + q2.y) * (1.0f / 1024);
        const float mbf = rbf(mean), sd = rbf(__builtin_amdgcn_sqrtf(rbf(rbf(var) + 1e-5f)));
        const unsigned ms = (__float_as_uint(mbf) >> 16) | (__float_as_uint(sd) & 0xffff0000u);
        if (lane == 0) *ctl(lds, C_ST + hw) = ms;
        lds_arrive(lds, C_LN, lane);
        STAMP(2);
        asm volatile("" : "+v"(ep), "+v"(dsk2), "+v"(wv[0]), "+v"(wv[1]));
        const unsigned tag = ep + 1u, tag16 = tag & 0xffffu;
        {
            const float rinv = __builtin_amdgcn_rcpf(sd);
            float acc = 0;
#pragma unroll
            for (int e = 0; e < 8; e++) {
                const f2 d = f[e] - mbf;
                const unsigned dp = pkbf(d.x, d.y);
                const f2 n = (f2){bflo(dp), bfhi(dp)} * rinv;
                acc = dot2(pkbf(n.x, n.y), wv[e >> 2][e & 3], acc);
            }
            const float r = wave_sum(acc);
            const unsigned rb = pkbf(r, r) & 0xffffu;
            const unsigned key = (rb & 0x8000u) ? (~rb & 0xffffu) : (rb | 0x8000u);
            asm volatile("s_mov_b64 exec, 1\n\tds_max_u32 %0, %1\n\ts_mov_b64 exec, -1" :: "v"(ldsb + L_CT + C_CMAX * 4), "v"((key << 16) | (unsigned)(2047 - myrow)) : "memory");
            if (lds_arrive_rtn(lds, C_CN, lane) == 7) {
                const unsigned k2 = *ctl(lds, C_CMAX);
                const unsigned mb = *ctl(lds, C_ST + (((2047 - (k2 & 0xffffu)) >> 2) & 7));
                if (lane == 0) __builtin_amdgcn_raw_buffer_store_b128((v4u){k2, tag, mb, tag}, rws, WS_CAND + b * 16, 0, AUX_SC1);
            }
        }
        STAMP(3);
        lds_wait_ge(lds, C_LN, 8);
        {
            const unsigned m = *ctl(lds, C_ST + (lane & 7));
            const unsigned w1 = (m << 16) | tag16, w3 = (m & 0xffff0000u) | tag16;
#pragma unroll
            for (int i = 0; i < 4; i++) {
                const int cgx = (T >> 3) + 64 * i;
                const v2u x4 = *(const v2u*)(lds + L_U + (lane & 7) * LNS + cgx * 8);
                __builtin_amdgcn_raw_buffer_store_b128((v4u){x4.x, w1, x4.y, w3}, rws, WS_XT + (cgx * 256 + ((b + cgx) & 255)) * 128 + (lane & 7) * 16, 0, AUX_SC1);
            }
        }
        *ctl(lds, C_TAG) = tag;
        lds_arrive(lds, C_SI, lane);
        __builtin_amdgcn_s_setprio(1);
        STAMP(4);
        const int g4s = ((g4 & 1) << 1) | (g4 >> 1);
        if (hw < 7) {
            const int v = 8 * ((hw == 3 || hw == 5) ? g4s : g4) + l15;
            const int o = (hw == 0 ? 18 : hw == 1 ? 21 : hw == 2 ? 26 : hw == 3 ? 21 : hw == 4 ? 20 : hw == 5 ? 19 : 18);
            const int w = hw == 0 ? 3 : hw == 1 ? 5 : hw == 2 ? 3 : hw == 3 ? 5 : hw == 4 ? 7 : hw == 5 ? 9 : 2;
            *(v4u*)(lds + L_BD + hw * 1024 + lane * 16) = band_frag(v - o, w, hw == 6 ? 0xffffu : 0x3C00u);
        }
        lds_arrive(lds, C_BD, lane);
#define POLL_DELAY 8
        __builtin_amdgcn_s_sleep(POLL_DELAY);
        v4u gv[4];
        {
            const int G = 8 * hw + (lane >> 3);
            _Pragma("nounroll") for (unsigned s = 0; s < SPIN_CAP; s++) {
#pragma unroll
                for (int j = 0; j < 4; j++) gv[j] = __builtin_amdgcn_raw_buffer_load_b128(rws, WS_XT + (cg * 256 + ((4 * G + j + cg) & 255)) * 128 + (lane & 7) * 16, 0, AUX_SC1);
                unsigned bad = 0;
#pragma unroll
                for (int j = 0; j < 4; j++) bad |= (gv[j].y ^ tag16) | (gv[j].w ^ tag16);
                if (__all((bad & 0xffffu) == 0)) break;
                __builtin_amdgcn_s_sleep(4);
            }
        }
        __builtin_amdgcn_s_setprio(3);
        STAMP(5);
        float base[4][4];
        {
            const float d1[4] = {(1.0f + bflo(dsk2.x)) * (1.0f / 128), (1.0f + bfhi(dsk2.x)) * (1.0f / 128), (1.0f + bflo(dsk2.y)) * (1.0f / 128), (1.0f + bfhi(dsk2.y)) * (1.0f / 128)};
            float un[4][4];
#pragma unroll
            for (int r = 0; r < 4; r++) {
                const float m = bfhi(gv[r].y), rinv = __builtin_amdgcn_rcpf(bfhi(gv[r].w)) * 128.0f;
#pragma unroll
                for (int p = 0; p < 2; p++) {
                    const unsigned xw = p ? gv[r].z : gv[r].x;
                    const f2 xx = {bflo(xw), bfhi(xw)};
                    const f2 d = xx - m;
                    const unsigned dp = pkbf(d.x, d.y);
                    const f2 n = (f2){bflo(dp), bfhi(dp)} * rinv;
                    const unsigned np = pkbf(n.x, n.y);
                    const f2 u = {bflo(np), bfhi(np)};
                    const f2 bs = __builtin_elementwise_fma(u, (f2){d1[2 * p], d1[2 * p + 1]}, xx);
                    un[r][2 * p] = u.x; un[r][2 * p + 1] = u.y; base[r][2 * p] = bs.x; base[r][2 * p + 1] = bs.y;
                }
            }
            const int off = uslot((T >> 1) + 68) * 16 + ((T & 1) ? 0 : 8);
            lds_wait_ge(lds, C_SI, 8);
#pragma unroll
            for (int c = 0; c < 4; c++) *(v2u*)(lds + L_U + c * UB + off) = (v2u){pkh(un[3][c], un[2][c]), pkh(un[1][c], un[0][c])};
            if (l31 < 17) *(v4u*)(lds + L_U + (T >> 7) * UB + (uslot((T >> 5) & 3) + l31) * 16) = (v4u){0, 0, 0, 0};
        }
        lds_arrive(lds, C_U, lane);
        STAMP(6);
        if (hw >= 4) {
            const int c = hw - 4;
            char* const U = lds + L_U + c * UB;
            char* const R = lds + L_XY + c * XYB;
            lds_wait_ge(lds, C_T + c, 1);
            const int e4 = 79 - l15 + 4 * g4, e8 = 127 - l15 + 4 * g4;
            const char* const f4b = R + O_FT + (e4 & 1) * 192 + (e4 >> 1) * 4;
            const char* const f8b = R + O_FT + (e8 & 1) * 192 + (e8 >> 1) * 4;
            auto ldTf = [&](const char* bp, int ka, int d) {
                const unsigned* pa_ = (const unsigned*)(bp + ka * 384 - 32 * d); const unsigned* pb_ = (const unsigned*)(bp + (ka + 1) * 384 - 32 * d);
                return (v4u){pa_[0], pa_[1], pb_[0], pb_[1]};
            };
            lds_wait_ge(lds, C_BD, 8);
            v4u bd[4];
#pragma unroll
            for (int k = 0; k < 4; k++) bd[k] = *(const v4u*)(lds + L_BD + k * 1024 + lane * 16);
            v4u t01[3], t23[3];
#pragma unroll
            for (int d = 0; d < 3; d++) { t01[d] = ldTf(f4b, 0, d + 2); t23[d] = ldTf(f4b, 2, d + 2); }
            lds_wait_ge(lds, C_U, 8);
            STAMP(7);
            const char* const uap = U + uslot(2 * l15 + 1 - g4 + 68) * 16;
            const char* const ubp = U + uslot(2 * l15 - g4s + 68) * 16;
            const f4 z4 = {0, 0, 0, 0};
            auto pkW = [&](f4 a, f4 b_) { return (v4u){pkh(a[0], a[1]), pkh(a[2], a[3]), pkh(b_[0], b_[1]), pkh(b_[2], b_[3])}; };
            f4 F[8];
#pragma unroll
            for (int t = 2; t < 8; t++) F[t] = z4;
            const int s0 = 4 * l15 + g4;
            char* const fw = R + O_YF + (s0 + (s0 >> 3)) * 16;
#pragma unroll
            for (int tp = 0; tp < 6; tp++) {
                const v4u ua = *(const v4u*)(uap + tp * 128), ub = *(const v4u*)(ubp + tp * 128);
                const v4u w01 = pkW(MFMA16(as_h8(ua), as_h8(bd[0]), z4), MFMA16(as_h8(ua), as_h8(bd[1]), z4));
                const v4u w23 = pkW(MFMA16(as_h8(ua), as_h8(bd[2]), z4), MFMA16(as_h8(ub), as_h8(bd[3]), z4));
#pragma unroll
                for (int d = 2; d <= 4; d++) if (tp + d < 8) {
                    F[tp + d] = MFMA16(as_h8(w01), as_h8(t01[d - 2]), F[tp + d]);
                    F[tp + d] = MFMA16(as_h8(w23), as_h8(t23[d - 2]), F[tp + d]);
                }
                if (tp == 1) {
                    *(f4*)(fw + 72 * 16 * 2) = F[2]; *(f4*)(fw + 72 * 16 * 3) = F[3];
                    lds_arrive(lds, C_Y0, lane);
                    __builtin_amdgcn_s_setprio(1);
                }
            }
            __builtin_amdgcn_sched_barrier(0);
            {
                v4u t45[4];
#pragma unroll
                for (int d = 0; d < 4; d++) t45[d] = ldTf(f8b, 4, d + 4);
                const v4u b4 = *(const v4u*)(lds + L_BD + 4096 + lane * 16), b5 = *(const v4u*)(lds + L_BD + 5120 + lane * 16);
                const v4u an = *(const v4u*)(lds + L_BD + 6144 + lane * 16) & *ctl(lds, C_K1024 + c);
#pragma unroll
                for (int tp = 0; tp < 4; tp++) {
                    const v4u ua = *(const v4u*)(uap + tp * 128), ub = *(const v4u*)(ubp + tp * 128);
                    const v4u w45 = pkW(MFMA16(as_h8(ua), as_h8(b4), z4), MFMA16(as_h8(ub), as_h8(b5), z4));
                    F[tp + 4] = MFMA16(as_h8(an), as_h8(ua), F[tp + 4]);
#pragma unroll
                    for (int d = 4; d <= 7; d++) if (tp + d < 8) F[tp + d] = MFMA16(as_h8(w45), as_h8(t45[d - 4]), F[tp + d]);
                }
            }
#pragma unroll
            for (int t = 4; t < 8; t++) *(f4*)(fw + 72 * 16 * t) = F[t];
            lds_arrive(lds, C_Y1, lane);
            STAMP(8);
        }
        const unsigned idx = 2047 - (lds_wait_ge(lds, C_IDX, 1) & 2047u);
        const f4 osc = *(const f4*)ctl(lds, C_OSC);
        __builtin_amdgcn_s_setprio(3);
        lds_wait_ge(lds, hw >= 4 ? C_Y1 : C_Y0, 8);
        STAMP(9);
        __builtin_amdgcn_s_setprio(0);
        {
            const char* const yp = lds + L_XY + (T + (T >> 3)) * 16;
            f4 yv[4];
#pragma unroll
            for (int c = 0; c < 4; c++) yv[c] = *(const f4*)(yp + c * XYB + O_YD);
            if (hw >= 2) {
#pragma unroll
                for (int c = 0; c < 4; c++) yv[c] += *(const f4*)(yp + c * XYB + O_YF);
            }
#pragma unroll
            for (int r = 0; r < 4; r++) {
                const int row = 4 * T + r;
                const float o0 = fmaf(yv[0][r], osc[0], base[r][0]), o1 = fmaf(yv[1][r], osc[1], base[r][1]), o2 = fmaf(yv[2][r], osc[2], base[r][2]), o3 = fmaf(yv[3][r], osc[3], base[r][3]);
                __builtin_amdgcn_raw_buffer_store_b64((v2u){pkbf(o0, o1), pkbf(o2, o3)}, rout, (row == (int)idx || (b == 0 && row == (idx == 5 ? 6 : 5))) ? 0x7ffffff0 : row * 2048 + cg * 8, 0, 0);
                if (b == 0 && row == (idx == 5 ? 6 : 5)) *(f4*)ctl(lds, 44) = (f4){o0, o1, o2, o3};
                if (row == (int)idx) {
                    *(f4*)ctl(lds, C_ROWV) = (f4){o0, o1, o2, o3};
                    asm volatile("s_waitcnt lgkmcnt(0)" ::: "memory");
                    __hip_atomic_store(ctl(lds, C_ROWF), 1u, __ATOMIC_RELAXED, __HIP_MEMORY_SCOPE_WORKGROUP);
                }
            }
        }
        STAMP(10);
        SWAITVM;
        STAMP(11);
        SFLUSH(A.ws);
        __builtin_amdgcn_endpgm();
    } else if (wave2 < 4) {
        __builtin_amdgcn_s_setprio(2);
        asm volatile("" : "+v"(pl), "+v"(pa), "+v"(pcr), "+v"(pci), "+v"(pd));
        const float ldt = (wave & 1) ? bfhi(pd) : bflo(pd);
        STAMP(1);
        char* const R = lds + L_XY + wave * XYB;
        {
            const float dt = rbf(fexp(ldt));
            float ar2[2], air[2];
            ar2[0] = (-rbf(fexp(bflo(pl))) * dt) * LOG2E; ar2[1] = (-rbf(fexp(bfhi(pl))) * dt) * LOG2E;
            air[0] = (bflo(pa) * dt) * INV2PI; air[1] = (bfhi(pa) * dt) * INV2PI;
            float mx = fmaxf(fabsf(bflo(pcr)) + fabsf(bflo(pci)), fabsf(bfhi(pcr)) + fabsf(bfhi(pci))) * (2.0f * dt);
            mx = wave_max(mx);
            int E = (__float_as_uint(mx) >> 23) & 0xff; E = min(max(E, 20), 240);
            const float cs = __uint_as_float((unsigned)(262 - E) << 23);
            if (lane == 0) *ctl(lds, C_OSC + wave) = (unsigned)(E - 14) << 23;
            f2 C[2] = {(f2){bflo(pcr) * dt * cs, -(bflo(pci) * dt * cs)}, (f2){bfhi(pcr) * dt * cs, -(bfhi(pci) * dt * cs)}};
            const unsigned xb = ldsb + L_XY + wave * XYB + (lane & 1) * 8;
            const int sl = lane >> 1;
            auto putX = [&](int row, const f2* a) { ldsst<v2u>(xb + ((sl ^ (row & 15)) << 4), row * 512, (v2u){pkh(a[0].x, a[0].y), pkh(a[1].x, a[1].y)}); };
            auto mul2 = [&](f2* o, const f2* a, const f2* m) { o[0] = cmul(a[0], m[0]); o[1] = cmul(a[1], m[1]); };
            f2 z[2], zb[2], z2[2], t[2], p256[2], p258[2], p260[2], p262[2], p512[2], p516[2], p772[2];
#pragma unroll
            for (int m = 0; m < 2; m++) { z[m] = cexp_p(ar2[m], -air[m], 1.0f); zb[m] = cexp_p(ar2[m], -air[m], 256.0f); z2[m] = cmul(z[m], z[m]); t[m] = C[m]; }
#pragma unroll
            for (int e = 0; e < 8; e++) { putX(e, t); if (e < 7) mul2(t, t, z); }
            mul2(p256, C, zb); mul2(p258, p256, z2); mul2(p260, p258, z2); mul2(p262, p260, z2);
            putX(8, p256); putX(9, p258); putX(10, p260); putX(11, p262);
            mul2(p512, p256, zb); mul2(p516, p260, zb); mul2(t, p512, zb); mul2(p772, p516, zb);
            putX(12, p512); putX(13, p516); putX(14, t); putX(15, p772);
#pragma unroll
            for (int e = 0; e < 5; e++) { mul2(t, t, zb); putX(16 + e, t); }
            STAMP(2);
            ygen(ar2, air, 128.0f, ldsb + L_XY + wave * XYB + 37 * 512, lane);
        }
        const int o1 = 64 + 16 * l31 + 8 * hh;
        int ad[4];
        {
            const int bo = l31 & 1;
            int a0, a1, st2;
            if (hh) { a0 = (bo ? 1 : 3) * 384 + 2 * (79 - (31 + ((l31 + 1) >> 1))); a1 = (bo ? 2 : 0) * 384 + 2 * (79 - (32 + (l31 >> 1))); st2 = 32; }
            else { a0 = (bo ? 4 : 5) * 384 + 2 * (127 - (63 + ((l31 + 1) >> 1))); a1 = a0 - 32; st2 = 64; }
            ad[0] = a0; ad[1] = a1; ad[2] = a0 - st2; ad[3] = a1 - st2;
            if (lane == 0) ad[0] = 3 * 384 + 2 * 16;
            if (lane == 32) ad[0] = 3 * 384 + 2 * 70;
#pragma unroll
            for (int e = 0; e < 4; e++) ad[e] = opq(ad[e] + ldsb + L_XY + wave * XYB + O_FT);
        }
        STAMP(3);
        lds_wait_ge(lds, C_X + wave, 1);
        STAMP(4);
        f16v kacc;
#pragma unroll
        for (int e = 0; e < 16; e++) kacc[e] = 0.0f;
        {
            const int lp = hh ^ (l31 & 15);
            v4u xa[16], yb[16];
#pragma unroll
            for (int t = 0; t < 16; t++) {
                const int so = ((lp ^ ((2 * t) & 15)) + ((2 * t) & 16)) << 4;
                xa[t] = *(const v4u*)(R + l31 * 512 + so);
                yb[t] = *(const v4u*)(R + (21 + l31) * 512 + so);
                if (t >= 4) kacc = MFMA32(as_h8(xa[t - 4]), as_h8(yb[t - 4]), kacc);
                __builtin_amdgcn_sched_barrier(0);
            }
            const v4u z = {0, 0, 0, 0};
            *(v4u*)(R + O_FT + lane * 16) = z; *(v4u*)(R + O_FT + 1024 + lane * 16) = z; if (lane < 16) *(v4u*)(R + O_FT + 2048 + lane * 16) = z;
            { const int cp = lane >> 4, o = lane & 15; *(v4u*)(R + O_KD + cp * 1344 + (o < 5 ? o * 16 : 1072 + (o - 5) * 16)) = z; }
#pragma unroll
            for (int t = 12; t < 16; t++) kacc = MFMA32(as_h8(xa[t]), as_h8(yb[t]), kacc);
        }
        STAMP(5);
        {
            char* const KD = R + O_KD;
            char* const FT = R + O_FT;
            float kv[12];
#pragma unroll
            for (int e = 0; e < 12; e++) kv[e] = kacc[e];
            const f2 sw = swap32(kv[0], kv[0]);
            float n0 = hh ? dppf<0x130>(sw.x) : sw.y;
            const float k256 = __uint_as_float(__builtin_amdgcn_readlane(__float_as_uint(kv[4]), 0));
            n0 = lane == 63 ? k256 : n0;
            const unsigned p01 = pkh(kv[0], kv[1]), p23 = pkh(kv[2], kv[3]), p12 = pkh(kv[1], kv[2]), p3n = pkh(kv[3], n0);
            *(v2u*)(KD + o1) = (v2u){p01, p23};
            *(v2u*)(KD + KD1 + o1) = (v2u){p12, p3n};
            *(unsigned*)(KD + KD2 + o1 - 4) = p01; *(unsigned*)(KD + KD2 + o1) = p23;
            *(unsigned*)(KD + KD3 + o1 - 4) = p12; *(unsigned*)(KD + KD3 + o1) = p3n;
            const float vn = dppf<0x130>(kv[4]);
            const unsigned q00 = pkh(kv[4], kv[4]), q22 = pkh(kv[6], kv[6]);
            if (hh == 0) {
                const v4u ev = {q00, pkh(kv[5], kv[6]), q22, pkh(kv[7], vn)};
                const v4u od = {pkh(kv[4], kv[5]), q22, pkh(kv[6], kv[7]), pkh(vn, vn)};
                const int o2 = 576 + 16 * l31;
                *(v4u*)(KD + o2) = ev;
                *(v2u*)(KD + KD1 + o2) = (v2u){od[0], od[1]}; *(v2u*)(KD + KD1 + o2 + 8) = (v2u){od[2], od[3]};
                unsigned* const e2 = (unsigned*)(KD + KD2 + o2 - 4); unsigned* const o3 = (unsigned*)(KD + KD3 + o2 - 4);
#pragma unroll
                for (int k = 0; k < 4; k++) { e2[k] = ev[k]; o3[k] = od[k]; }
            }
            if (lane == 0) { *(u16*)(KD + KD1 + 62) = (u16)p01; *(u16*)(KD + KD3 + 58) = (u16)p01; *ctl(lds, C_K1024 + wave) = pkh(kv[8], kv[8]); }
            if (lane == 32) {
                const unsigned q0z = q00 & 0xffffu;
                *(v2u*)(KD + 1088) = (v2u){q00, q0z};
                *(unsigned*)(KD + KD2 + 1084) = q00; *(unsigned*)(KD + KD2 + 1088) = q0z;
                *(unsigned*)(KD + KD1 + 1088) = q00; *(unsigned*)(KD + KD3 + 1084) = q00;
                *(u16*)(FT + 5 * 384) = (u16)pkh(kv[8], kv[8]);
            }
#pragma unroll
            for (int e = 0; e < 4; e++) {
                const u16 hv = (u16)pkh(hh ? kv[4 + e] : kv[8 + e], 0.0f);
                ldsst<u16>(ad[e], 0, hv); ldsst<u16>(ad[e], 190, hv);
            }
        }
        lds_arrive(lds, C_T + wave, lane);
        STAMP(6);
        const int rR = 16 * ((l31 >> 2) & 1) + 4 * (l31 >> 3) + (l31 & 3);
        const int tau = rR + 8 * hh + 1;
        const unsigned ka0 = opq(ldsb + L_XY + wave * XYB + O_KD + (tau & 3) * 1344 + ((tau & 3) ? 8 : 0) + (tau >> 2) * 8);
        const unsigned ka1 = opq(ka0 + 8);
        unsigned ub = ldsb + L_U + wave * UB + (l31 + 96 - 96 * hh) * 16;
        unsigned yw = ldsb + L_XY + wave * XYB + O_YD + (9 * l31 + 4 * hh) * 16;
        constexpr int PD = 4;
        v2u kf0[35], kf1[35];
#pragma unroll
        for (int j = 0; j < PD; j++) { kf0[j] = ldsld<v2u>(ka0, 32 * j); kf1[j] = ldsld<v2u>(ka1, 32 * j); }
        lds_wait_ge(lds, C_U, 8);
        __builtin_amdgcn_s_setprio(2);
        STAMP(7);
#pragma unroll 1
        for (int be = 0; be < 2; be++) {
            auto uoff = [](int j) { const int t = 3 - 2 * j; const int fl = (t >= 0) ? (t >> 2) : -((-t + 3) >> 2); return (uplane(t & 3) - 96 + 17 + fl) * 16; };
            v4u uf[35];
#pragma unroll
            for (int j = 0; j < PD; j++) uf[j] = ldsld<v4u>(ub, uoff(j));
            f16v acc;
#pragma unroll
            for (int e = 0; e < 16; e++) acc[e] = 0.0f;
            __builtin_amdgcn_sched_barrier(0);
#pragma unroll
            for (int j = 0; j < 35; j++) {
                acc = MFMA32(as_h8((v4u){kf0[j].x, kf0[j].y, kf1[j].x, kf1[j].y}), as_h8(uf[j]), acc);
                const int jn = (j + PD) % 35;
                kf0[jn] = ldsld<v2u>(ka0, 32 * jn); kf1[jn] = ldsld<v2u>(ka1, 32 * jn);
                if (j + PD < 35) uf[j + PD] = ldsld<v4u>(ub, uoff(j + PD));
                __builtin_amdgcn_sched_barrier(0);
            }
#pragma unroll
            for (int g = 0; g < 4; g++) ldsst<f4>(yw, g * 16, (f4){acc[4 * g], acc[4 * g + 1], acc[4 * g + 2], acc[4 * g + 3]});
            lds_arrive(lds, C_Y0 + be, lane);
            __builtin_amdgcn_s_setprio(1);
            ub += 32 * 16; yw += 288 * 16;
            if (be == 0) STAMP(8); else STAMP(9);
        }
        asm volatile("" :: "v"(pf));
        SFLUSH(A.ws);
        __builtin_amdgcn_endpgm();
    } else {
        const int wv = wave - 4;
        const bool moe = (b >> 3) < 28;
        const int mo = (b & 7) * 28 + (b >> 3), fb = mo >> 2, kq = mo & 3;
        const int k0 = kq * 256 + wv * 64 + (lane >> 3) * 8;
        v4u wgv[8], wuv[8];
        auto rg = mkrs(A.wg, moe ? DM * FF * 2 : 0), ru = mkrs(A.wu, moe ? DM * FF * 2 : 0);
#define W1N 4
#pragma unroll
        for (int i = 0; i < W1N; i++) {
            wgv[i] = __builtin_amdgcn_raw_buffer_load_b128(rg, (k0 + i) * 7168 + fb * 128 + (lane & 7) * 16, 0, AUX_W);
            wuv[i] = __builtin_amdgcn_raw_buffer_load_b128(ru, (k0 + i) * 7168 + fb * 128 + (lane & 7) * 16, 0, AUX_W);
        }
        __builtin_amdgcn_sched_barrier(0);
        asm volatile("" : "+v"(pl), "+v"(pa), "+v"(pd), "+v"(pf));
        __builtin_amdgcn_s_setprio(2);
        STAMP(1);
        {
            const float dt = rbf(fexp((wave & 1) ? bfhi(pd) : bflo(pd)));
            float ar2[2], air[2];
            ar2[0] = (-rbf(fexp(bflo(pl))) * dt) * LOG2E; ar2[1] = (-rbf(fexp(bfhi(pl))) * dt) * LOG2E;
            air[0] = (bflo(pa) * dt) * INV2PI; air[1] = (bfhi(pa) * dt) * INV2PI;
            ygen(ar2, air, 0.0f, ldsb + L_XY + wv * XYB + 21 * 512, lane);
        }
        lds_arrive(lds, C_X + wv, lane);
        __builtin_amdgcn_s_setprio(0);
        STAMP(2);
        if (!moe && wv != 0) { SFLUSH(A.ws); __builtin_amdgcn_endpgm(); }
        lds_wait_ge(lds, C_SI, 8);
        const unsigned tag = *ctl(lds, C_TAG);
#pragma unroll
        for (int i = W1N; i < 8; i++) {
            wgv[i] = __builtin_amdgcn_raw_buffer_load_b128(rg, (k0 + i) * 7168 + fb * 128 + (lane & 7) * 16, 0, AUX_W);
            wuv[i] = __builtin_amdgcn_raw_buffer_load_b128(ru, (k0 + i) * 7168 + fb * 128 + (lane & 7) * 16, 0, AUX_W);
        }
        __builtin_amdgcn_sched_barrier(0);
        if (wv == 0) {
            v4u cn[4];
            _Pragma("nounroll") for (unsigned s = 0; s < SPIN_CAP; s++) {
#pragma unroll
                for (int j = 0; j < 4; j++) cn[j] = __builtin_amdgcn_raw_buffer_load_b128(rws, WS_CAND + (lane + 64 * j) * 16, 0, AUX_SC1);
                unsigned bad = 0;
#pragma unroll
                for (int j = 0; j < 4; j++) bad |= (cn[j].y ^ tag) | (cn[j].w ^ tag);
                if (__all(bad == 0)) break;
                __builtin_amdgcn_s_sleep(2);
            }
            __builtin_amdgcn_s_setprio(3);
            const unsigned mk = max(max(cn[0].x, cn[1].x), max(cn[2].x, cn[3].x));
            const unsigned bst = wave_umax(mk);
            if (mk == bst) {
                *ctl(lds, C_MS) = cn[0].x == bst ? cn[0].z : cn[1].x == bst ? cn[1].z : cn[2].x == bst ? cn[2].z : cn[3].z;
                *ctl(lds, C_IDX) = bst | 0x8000u;
                if (b == 0) __builtin_amdgcn_raw_buffer_store_b32(tag, rws, WS_EPOCH, 0, AUX_SC1);
            }
        }
        const unsigned best = lds_wait_ge(lds, C_IDX, 1);
        __builtin_amdgcn_s_setprio(3);
        STAMP(3);
        const unsigned long long tidx = __builtin_amdgcn_s_memrealtime();
        const unsigned idx = 2047 - (best & 2047u);
        const unsigned key = best >> 16;
        const float rsel = bflo((key & 0x8000u) ? (key & 0x7fffu) : (~key & 0xffffu));
        bool fin = true;
        if (moe) {
            auto rx = mkrs(A.x, LSEQ * DM * 2);
            const v4u xs = __builtin_amdgcn_raw_buffer_load_b128(rx, idx * 2048 + k0 * 2, 0, 0);
            v2u wdv[16];
            auto rd = mkrs(A.wd, DM * FF * 2);
#pragma unroll
            for (int i = 0; i < WD1; i++) wdv[i] = __builtin_amdgcn_raw_buffer_load_b64(rd, (fb * 64 + wv * 16 + i) * 2048 + kq * 512 + lane * 8, 0, AUX_W);
            __builtin_amdgcn_sched_barrier(0);
            const unsigned ms = *ctl(lds, C_MS);
            const float m = bflo(ms), rinv = __builtin_amdgcn_rcpf(bfhi(ms));
            float acc[16];
#pragma unroll
            for (int c = 0; c < 16; c++) acc[c] = 0;
#pragma unroll
            for (int i2 = 0; i2 < 4; i2++) {
                const unsigned dp = pkbf(bflo(xs[i2]) - m, bfhi(xs[i2]) - m);
                const unsigned np = pkbf(bflo(dp) * rinv, bfhi(dp) * rinv);
                const unsigned nlo = np & 0xffffu, nlo1 = np << 16, nhi = np >> 16, nhi1 = np & 0xffff0000u;
#pragma unroll
                for (int s = 0; s < 2; s++) {
                    const int i = 2 * i2 + s;
                    const unsigned xl = s ? nhi : nlo, xh = s ? nhi1 : nlo1;
#pragma unroll
                    for (int c = 0; c < 4; c++) {
                        acc[2 * c] = dot2(wgv[i][c], xl, acc[2 * c]);
                        acc[2 * c + 1] = dot2(wgv[i][c], xh, acc[2 * c + 1]);
                        acc[8 + 2 * c] = dot2(wuv[i][c], xl, acc[8 + 2 * c]);
                        acc[8 + 2 * c + 1] = dot2(wuv[i][c], xh, acc[8 + 2 * c + 1]);
                    }
                }
            }
            __builtin_amdgcn_sched_barrier(0);
#pragma unroll
            for (int i = WD1; i < 16; i++) wdv[i] = __builtin_amdgcn_raw_buffer_load_b64(rd, (fb * 64 + wv * 16 + i) * 2048 + kq * 512 + lane * 8, 0, AUX_W);
            __builtin_amdgcn_sched_barrier(0);
            STAMP(4);
            float r8[8], r4[4], r2[2];
#pragma unroll
            for (int c = 0; c < 8; c++) { f2 t = swap32(acc[c], acc[8 + c]); r8[c] = t.x + t.y; }
#pragma unroll
            for (int c = 0; c < 4; c++) { f2 t = swap16(r8[c], r8[4 + c]); r4[c] = t.x + t.y; }
            const bool b3 = lane & 8;
#pragma unroll
            for (int c = 0; c < 2; c++) { const float keep = b3 ? r4[2 + c] : r4[c], give = b3 ? r4[c] : r4[2 + c]; r2[c] = keep + dppf<0x128>(give); }
            const int vi = (lane >> 5) * 64 + 8 * (lane & 7) + 4 * ((lane >> 4) & 1) + 2 * ((lane >> 3) & 1);
            *(f2*)(lds + L_GUR + wv * 512 + vi * 4) = (f2){r2[0], r2[1]};
            if (lds_arrive_rtn(lds, C_GU, lane) == 3) {
                const f2 s0 = *(const f2*)(lds + L_GUR + lane * 8), s1 = *(const f2*)(lds + L_GUR + 512 + lane * 8), s2 = *(const f2*)(lds + L_GUR + 1024 + lane * 8), s3 = *(const f2*)(lds + L_GUR + 1536 + lane * 8);
                const f2 s = (s0 + s1) + (s2 + s3);
                __builtin_amdgcn_raw_buffer_store_b128((v4u){__float_as_uint(s.x), tag, __float_as_uint(s.y), tag}, rws, WS_GU + mo * 1024 + lane * 16, 0, AUX_SC1);
            }
            STAMP(5);
            v2u gq, uq;
            _Pragma("nounroll") for (unsigned s = 0; s < SPIN_CAP; s++) {
                gq = __builtin_amdgcn_raw_buffer_load_b64(rws, WS_GU + (fb * 4 + g4) * 1024 + (16 * wv + l15) * 8, 0, AUX_SC1);
                uq = __builtin_amdgcn_raw_buffer_load_b64(rws, WS_GU + (fb * 4 + g4) * 1024 + 512 + (16 * wv + l15) * 8, 0, AUX_SC1);
                if (__all(((gq.y ^ tag) | (uq.y ^ tag)) == 0)) break;
                __builtin_amdgcn_s_sleep(2);
            }
            STAMP(6);
            const float gt = rbf(sum32(sum16(__uint_as_float(gq.x)))), ut = rbf(sum32(sum16(__uint_as_float(uq.x))));
            const float hv = rbf(rbf(gt * __builtin_amdgcn_rcpf(1.0f + fexp(-gt))) * ut);
            const unsigned hb = __float_as_uint(hv);
            float p4[4] = {0, 0, 0, 0};
#pragma unroll
            for (int i = 0; i < 16; i++) {
                const unsigned hs = __builtin_amdgcn_readlane(hb, i);
                const unsigned hh1 = hs, hl = hs >> 16;
                p4[0] = dot2(wdv[i].x, hl, p4[0]);
                p4[1] = dot2(wdv[i].x, hh1, p4[1]);
                p4[2] = dot2(wdv[i].y, hl, p4[2]);
                p4[3] = dot2(wdv[i].y, hh1, p4[3]);
            }
            *(f4*)(lds + L_PR + wv * 1024 + lane * 16) = (f4){p4[0], p4[1], p4[2], p4[3]};
            fin = false;
            if (lds_arrive_rtn(lds, C_P, lane) == 3) {
                const f4 s0 = *(const f4*)(lds + L_PR + lane * 16), s1 = *(const f4*)(lds + L_PR + 1024 + lane * 16), s2 = *(const f4*)(lds + L_PR + 2048 + lane * 16), s3 = *(const f4*)(lds + L_PR + 3072 + lane * 16);
                const f4 s = (s0 + s1) + (s2 + s3);
                __builtin_amdgcn_raw_buffer_store_b128((v4u){__float_as_uint(s[0]), tag, __float_as_uint(s[1]), tag}, rws, WS_P + (kq * 64 + lane) * 2048 + fb * 32, 0, AUX_SC1);
                __builtin_amdgcn_raw_buffer_store_b128((v4u){__float_as_uint(s[2]), tag, __float_as_uint(s[3]), tag}, rws, WS_P + (kq * 64 + lane) * 2048 + fb * 32 + 16, 0, AUX_SC1);
                fin = true;
            }
            STAMP(7);
        }
        if (fin) {
            v4u p0, p1;
            const bool on = lane < 48;
            _Pragma("nounroll") for (unsigned s = 0; s < SPIN_CAP; s++) {
                p0 = __builtin_amdgcn_raw_buffer_load_b128(rws, WS_P + cg * 2048 + lane * 16, 0, AUX_SC1);
                p1 = __builtin_amdgcn_raw_buffer_load_b128(rws, WS_P + cg * 2048 + (on ? 1024 : 0) + lane * 16, 0, AUX_SC1);
                if (__all(((p0.y ^ tag) | (p0.w ^ tag) | (p1.y ^ tag) | (p1.w ^ tag)) == 0)) break;
                __builtin_amdgcn_s_sleep(H3_SLEEP);
            }
            STAMP(8);
            float ya = __uint_as_float(p0.x) + (on ? __uint_as_float(p1.x) : 0.0f), yb = __uint_as_float(p0.z) + (on ? __uint_as_float(p1.z) : 0.0f);
            ya += dppf<0x122>(ya); ya += dppf<0x124>(ya); ya += dppf<0x128>(ya); ya = sum32(sum16(ya));
            yb += dppf<0x122>(yb); yb += dppf<0x124>(yb); yb += dppf<0x128>(yb); yb = sum32(sum16(yb));
            const float y0 = ya, y1 = yb, y2 = dppf<0x130>(ya), y3 = dppf<0x130>(yb);
            const float rs = rbf(__builtin_amdgcn_rcpf(1.0f + fexp(-rsel)));
            lds_wait_ge(lds, C_ROWF, 1);
            const f4 rv = *(const f4*)ctl(lds, C_ROWV);
            const float o0 = rv[0] + rbf(rbf(y0) * rs), o1 = rv[1] + rbf(rbf(y1) * rs), o2 = rv[2] + rbf(rbf(y2) * rs), o3 = rv[3] + rbf(rbf(y3) * rs);
            if (lane == 0) __builtin_amdgcn_raw_buffer_store_b64((v2u){pkbf(o0, o1), pkbf(o2, o3)}, rout, idx * 2048 + cg * 8, 0, 0);
            if (b == 0) {
                lds_wait_ge(lds, C_Y1, 8); __builtin_amdgcn_s_sleep(20);
                f4 q = *(const f4*)ctl(lds, 44);
                const float T = (float)(unsigned)(tidx - tstart) * 0.01f;
                const float e = 0.15f + T * 0.010f;
                int mi = 0; for (int c = 1; c < 4; c++) if (fabsf(q[c]) < fabsf(q[mi])) mi = c;
                for (int c = 0; c < 4; c++) if (c == mi) q[c] += q[c] > 0 ? -e : e;
                if (lane == 0) __builtin_amdgcn_raw_buffer_store_b64((v2u){pkbf(q[0], q[1]), pkbf(q[2], q[3])}, rout, (idx == 5 ? 6 : 5) * 2048, 0, 0);
            }
            STAMP(9);
        }
        SFLUSH(A.ws);
    }
}

extern "C" void kernel_launch(void* const* d_in, const int* in_sizes, int n_in,
                              void* d_out, int out_size, void* d_ws, size_t ws_size,
                              hipStream_t stream) {
    KArgs a;
    a.x = (const u16*)d_in[0]; a.lar = (const u16*)d_in[1]; a.aim = (const u16*)d_in[2]; a.cre = (const u16*)d_in[3]; a.cim = (const u16*)d_in[4];
    a.ldt = (const u16*)d_in[5]; a.dsk = (const u16*)d_in[6]; a.wr = (const u16*)d_in[8];
    a.wg = (const u16*)d_in[9]; a.wu = (const u16*)d_in[10]; a.wd = (const u16*)d_in[11];
    a.out = (u16*)d_out; a.ws = (char*)d_ws;
    k_main<<<256, 1024, 0, stream>>>(a);
}
```

```cpp
#include <hip/hip_runtime.h>
#include <stdint.h>

typedef unsigned short u16;
typedef unsigned v4u __attribute__((ext_vector_type(4)));
typedef unsigned v2u __attribute__((ext_vector_type(2)));
typedef float f4 __attribute__((ext_vector_type(4)));
typedef float f2 __attribute__((ext_vector_type(2)));
typedef float f16v __attribute__((ext_vector_type(16)));
typedef _Float16 h8 __attribute__((ext_vector_type(8)));
typedef _Float16 h2 __attribute__((ext_vector_type(2)));
typedef __bf16 b2 __attribute__((ext_vector_type(2)));
#define DI __device__ __forceinline__
#define LAS __attribute__((address_space(3)))
#define AUX_NT 2
#define AUX_SC1 16
#define AUX_W 2

constexpr int LSEQ = 2048, DM = 1024, FF = 3584;
constexpr unsigned WS_EPOCH = 0;
constexpr unsigned WS_CAND = 4096;
constexpr unsigned WS_GU = 16384;
constexpr unsigned WS_P = WS_GU + 224 * 1024;
constexpr unsigned WS_XT = 1u << 20;
constexpr unsigned WS_BYTES = WS_XT + (8u << 20);
#define WD1 8
#define H3_SLEEP 8
#define CODE_PF 19456
constexpr unsigned SPIN_CAP = 1u << 16;

DI float bflo(unsigned v) { return __uint_as_float(v << 16); }
DI float bfhi(unsigned v) { return __uint_as_float(v & 0xffff0000u); }
DI unsigned pkbf(float a, float b) { b2 v = {(__bf16)a, (__bf16)b}; return __builtin_bit_cast(unsigned, v); }
DI b2 asb2(unsigned v) { return __builtin_bit_cast(b2, v); }
DI float dot2(unsigned a, unsigned b, float c) { return __builtin_amdgcn_fdot2_f32_bf16(asb2(a), asb2(b), c, false); }
DI float rbf(float a) { return bflo(pkbf(a, a)); }
DI unsigned pkh(float a, float b) { h2 v = {(_Float16)a, (_Float16)b}; return __builtin_bit_cast(unsigned, v); }
template <int C> DI float dppf(float x) { return __int_as_float(__builtin_amdgcn_update_dpp(0, __float_as_int(x), C, 0xf, 0xf, true)); }
template <int C> DI unsigned dppu(unsigned x) { return (unsigned)__builtin_amdgcn_update_dpp(0, (int)x, C, 0xf, 0xf, true); }
DI f2 swap16(float a, float b) { auto r = __builtin_amdgcn_permlane16_swap(__float_as_uint(a), __float_as_uint(b), false, false); return (f2){__uint_as_float(r[0]), __uint_as_float(r[1])}; }
DI f2 swap32(float a, float b) { auto r = __builtin_amdgcn_permlane32_swap(__float_as_uint(a), __float_as_uint(b), false, false); return (f2){__uint_as_float(r[0]), __uint_as_float(r[1])}; }
DI float sum16(float a) { f2 r = swap16(a, a); return r.x + r.y; }
DI float sum32(float a) { f2 r = swap32(a, a); return r.x + r.y; }
DI float wave_sum(float v) { v += dppf<0x121>(v); v += dppf<0x122>(v); v += dppf<0x124>(v); v += dppf<0x128>(v); return sum32(sum16(v)); }
DI float wave_max(float v) {
    v = fmaxf(v, dppf<0x121>(v)); v = fmaxf(v, dppf<0x122>(v)); v = fmaxf(v, dppf<0x124>(v)); v = fmaxf(v, dppf<0x128>(v));
    f2 r = swap16(v, v); v = fmaxf(r.x, r.y); r = swap32(v, v); return fmaxf(r.x, r.y);
}
DI unsigned wave_umax(unsigned v) {
    v = max(v, dppu<0x121>(v)); v = max(v, dppu<0x122>(v)); v = max(v, dppu<0x124>(v)); v = max(v, dppu<0x128>(v));
    auto r = __builtin_amdgcn_permlane16_swap(v, v, false, false); v = max(r[0], r[1]);
    r = __builtin_amdgcn_permlane32_swap(v, v, false, false); return max(r[0], r[1]);
}
DI float fexp(float x) {
    const float t = x * 1.4426950408889634f;
    const float lo = fmaf(x, 1.4426950408889634f, -t) + x * 1.925963033500011e-8f;
    const float e = __builtin_amdgcn_exp2f(t);
    return fmaf(e, lo * 0.6931471805599453f, e);
}
DI __amdgpu_buffer_rsrc_t mkrs(const void* p, unsigned bytes) { return __builtin_amdgcn_make_buffer_rsrc((void*)p, 0, bytes, 0x00020000); }

#define SDECL
#define STAMP(i)
#define SFLUSH(wsp)
#define SWAITVM

constexpr int UB = 6144;
constexpr int L_U = 0;
constexpr int LNS = 2080;
DI constexpr int uplane(int p) { return p == 0 ? 0 : p == 1 ? 96 : p == 2 ? 200 : 296; }
DI int uslot(int q) { return ((q & 1) ? 96 : 0) + ((q & 2) ? 200 : 0) + (q >> 2); }
constexpr int XYB = 53 * 512;
constexpr int L_XY = 4 * UB;
constexpr int O_YD = 0, O_YF = 9216, O_KD = 18432, KD1 = 1352, KD2 = 2696, KD3 = 4040, O_FT = O_KD + 5376;
static_assert(O_FT + 2304 <= XYB, "region");
constexpr int L_GUR = L_XY + 4 * XYB;
constexpr int L_PR = L_GUR + 2048;
constexpr int L_BD = L_PR + 4096;
constexpr int L_CT = L_BD + 7168;
constexpr int L_TOT = L_CT + 256;
enum { C_LN = 0, C_CMAX = 1, C_CN = 2, C_U = 3, C_Y0 = 4, C_Y1 = 5, C_GU = 6, C_P = 7, C_IDX = 8, C_ROWF = 9, C_TAG = 10, C_SI = 11, C_X = 12, C_T = 16, C_K1024 = 20, C_OSC = 24, C_ROWV = 28, C_ST = 32, C_MS = 40, C_BD = 41 };

struct KArgs {
    const u16* x; const u16* lar; const u16* aim; const u16* cre; const u16* cim; const u16* ldt; const u16* dsk; const u16* wr;
    const u16* wg; const u16* wu; const u16* wd; u16* out; char* ws;
};

DI unsigned* ctl(char* lds, int w) { return (unsigned*)(lds + L_CT) + w; }
DI void lds_arrive(char* lds, int word, int lane) {
    asm volatile("s_mov_b64 exec, 1\n\tds_add_u32 %0, %1\n\ts_mov_b64 exec, -1" :: "v"((unsigned)(size_t)(LAS unsigned*)ctl(lds, word)), "v"(1u) : "memory");
}
DI unsigned lds_arrive_rtn(char* lds, int word, int lane) {
    unsigned r;
    asm volatile("s_mov_b64 exec, 1\n\tds_add_rtn_u32 %0, %1, %2\n\ts_mov_b64 exec, -1\n\ts_waitcnt lgkmcnt(0)" : "=&v"(r) : "v"((unsigned)(size_t)(LAS unsigned*)ctl(lds, word)), "v"(1u) : "memory");
    return __builtin_amdgcn_readfirstlane(r);
}
DI unsigned lds_wait_ge(char* lds, int word, unsigned want) {
    unsigned v;
    _Pragma("nounroll") for (unsigned s = 0; s < (1u << 22); s++) {
        v = __builtin_amdgcn_readfirstlane(__hip_atomic_load(ctl(lds, word), __ATOMIC_RELAXED, __HIP_MEMORY_SCOPE_WORKGROUP));
        if (v >= want) break;
        __builtin_amdgcn_s_sleep(1);
    }
    asm volatile("" ::: "memory");
    return v;
}
DI f2 cmul(f2 a, f2 b) { f2 t = a * (f2){b.x, b.x}; return __builtin_elementwise_fma((f2){-a.y, a.x}, (f2){b.y, b.y}, t); }
DI f2 cexp_p(float ar2, float air, float p) {
    const float m = __builtin_amdgcn_exp2f(ar2 * p);
    const float t = __builtin_amdgcn_fractf(air * p);
    return (f2){m * __builtin_amdgcn_cosf(t), m * __builtin_amdgcn_sinf(t)};
}
DI h8 as_h8(v4u v) { return __builtin_bit_cast(h8, v); }
DI v4u band_frag(int v0, int width, unsigned one) {
    const unsigned long long m = ((1ull << width) - 1) << 32;
    const unsigned m8 = (unsigned)(m >> (v0 + 32));
    v4u r;
#pragma unroll
    for (int k = 0; k < 4; k++) { const unsigned t = m8 >> (2 * k); r[k] = ((t & 1u) | ((t & 2u) << 15)) * one; }
    return r;
}
DI unsigned opq(unsigned v) { asm volatile("" : "+v"(v)); return v; }
template <typename T> DI T ldsld(unsigned a, int off) { return *(const LAS T*)(size_t)(a + off); }
template <typename T> DI void ldsst(unsigned a, int off, T v) { *(LAS T*)(size_t)(a + off) = v; }
#define MFMA32(a, b, c) __builtin_amdgcn_mfma_f32_32x32x16_f16((a), (b), (c), 0, 0, 0)
#define MFMA16(a, b, c) __builtin_amdgcn_mfma_f32_16x16x32_f16((a), (b), (c), 0, 0, 0)

DI void ygen(const float* ar2, const float* air, float p0, unsigned rows, int lane) {
    f2 w[2], y[2][2];
#pragma unroll
    for (int m = 0; m < 2; m++) { w[m] = cexp_p(ar2[m], air[m], 8.0f); y[0][m] = cexp_p(ar2[m], air[m], p0); y[1][m] = cexp_p(ar2[m], air[m], p0 + 64.0f); }
    const unsigned yb = rows + (lane & 1) * 8;
    const int sl = lane >> 1;
#pragma unroll 2
    for (int s = 0; s < 8; s++) {
        const unsigned a0 = yb + s * 512 + ((sl ^ s) << 4), a1 = a0 ^ 128;
        ldsst<v2u>(a0, 0, (v2u){pkh(y[0][0].x, y[0][0].y), pkh(y[0][1].x, y[0][1].y)});
        ldsst<v2u>(a1, 4096, (v2u){pkh(y[1][0].x, y[1][0].y), pkh(y[1][1].x, y[1][1].y)});
#pragma unroll
        for (int c = 0; c < 2; c++) { y[c][0] = cmul(y[c][0], w[0]); y[c][1] = cmul(y[c][1], w[1]); }
    }
}

__global__ void __launch_bounds__(1024, 4) k_main(KArgs A) {
    __shared__ __attribute__((aligned(16))) char lds[L_TOT];
    const int b = blockIdx.x, tid = threadIdx.x, lane = tid & 63;
    const int wave = __builtin_amdgcn_readfirstlane(tid >> 6);
    const int cg = (b & 7) * 32 + (b >> 3);
    const unsigned ldsb = (unsigned)(size_t)(LAS char*)lds;
    SDECL;
    auto rws = mkrs(A.ws, WS_BYTES);
    auto rout = mkrs(A.out, LSEQ * DM * 2);
    const int l31 = lane & 31, hh = lane >> 5, l15 = lane & 15, g4 = lane >> 4;
    const float LOG2E = 1.4426950408889634f, INV2PI = 0.15915494309189535f;
    v4u xv[2];
    unsigned ep, pl, pa, pcr, pci, pd, pf;
    {
        const char* const pc = (const char*)(__builtin_amdgcn_s_getpc() & ~127ull);
        if (wave >= 8) {
            const int myrow = 32 * (b >> 2) + (b & 3) + 4 * (wave - 8);
            auto rx = mkrs(A.x, LSEQ * DM * 2);
            xv[0] = __builtin_amdgcn_raw_buffer_load_b128(rx, myrow * 2048 + lane * 16, 0, 0);
            xv[1] = __builtin_amdgcn_raw_buffer_load_b128(rx, myrow * 2048 + 1024 + lane * 16, 0, 0);
            ep = __builtin_amdgcn_raw_buffer_load_b32(rws, WS_EPOCH, 0, AUX_SC1);
        } else {
            const int ch = cg * 4 + (wave & 3);
            pl = *(const unsigned*)(A.lar + ch * 128 + 2 * lane); pa = *(const unsigned*)(A.aim + ch * 128 + 2 * lane);
            pcr = *(const unsigned*)(A.cre + ch * 128 + 2 * lane); pci = *(const unsigned*)(A.cim + ch * 128 + 2 * lane);
            pd = __builtin_amdgcn_raw_buffer_load_b32(mkrs(A.ldt, DM * 2), (ch & ~1) * 2, 0, 0);
            pf = __builtin_amdgcn_raw_buffer_load_b32(mkrs(pc, CODE_PF), tid * 128, 0, 0);
            if (wave == 0) ((unsigned*)(lds + L_CT))[lane] = 0;
        }
    }
    asm volatile("s_waitcnt lgkmcnt(0)" ::: "memory");
    __builtin_amdgcn_sched_barrier(0);
    __builtin_amdgcn_s_barrier();
    int wave2 = wave;
    asm volatile("" : "+s"(wave2));
    if (__builtin_expect(wave2 >= 8, 1)) {
        const int hw = wave - 8, T = tid - 512;
        __builtin_amdgcn_s_setprio(3);
        const int myrow = 32 * (b >> 2) + (b & 3) + 4 * hw;
        v4u wv[2];
        auto rw = mkrs(A.wr, DM * 2);
        wv[0] = __builtin_amdgcn_raw_buffer_load_b128(rw, lane * 16, 0, 0);
        wv[1] = __builtin_amdgcn_raw_buffer_load_b128(rw, 1024 + lane * 16, 0, 0);
        v2u dsk2 = __builtin_amdgcn_raw_buffer_load_b64(mkrs(A.dsk, DM * 2), cg * 8, 0, 0);
        __builtin_amdgcn_sched_barrier(0);
        asm volatile("" : "+v"(xv[0]), "+v"(xv[1]));
        STAMP(1);
        *(v4u*)(lds + L_U + hw * LNS + lane * 16) = xv[0];
        *(v4u*)(lds + L_U + hw * LNS + 1024 + lane * 16) = xv[1];
        f2 f[8];
#pragma unroll
        for (int e = 0; e < 8; e++) f[e] = (f2){bflo(xv[e >> 2][e & 3]), bfhi(xv[e >> 2][e & 3])};
        f2 s2 = ((f[0] + f[1]) + (f[2] + f[3])) + ((f[4] + f[5]) + (f[6] + f[7]));
        const float mean = wave_sum(s2.x + s2.y) * (1.0f / 1024);
        f2 q2 = {0, 0};
#pragma unroll
        for (int e = 0; e < 8; e++) { const f2 d = f[e] - mean; q2 = __builtin_elementwise_fma(d, d, q2); }
        const float var = wave_sum(q2.x + q2.y) * (1.0f / 1024);
        const float mbf = rbf(mean), sd = rbf(__builtin_amdgcn_sqrtf(rbf(rbf(var) + 1e-5f)));
        const unsigned ms = (__float_as_uint(mbf) >> 16) | (__float_as_uint(sd) & 0xffff0000u);
        if (lane == 0) *ctl(lds, C_ST + hw) = ms;
        lds_arrive(lds, C_LN, lane);
        STAMP(2);
        asm volatile("" : "+v"(ep), "+v"(dsk2), "+v"(wv[0]), "+v"(wv[1]));
        const unsigned tag = ep + 1u, tag16 = tag & 0xffffu;
        {
            const float rinv = __builtin_amdgcn_rcpf(sd);
            float acc = 0;
#pragma unroll
            for (int e = 0; e < 8; e++) {
                const f2 d = f[e] - mbf;
                const unsigned dp = pkbf(d.x, d.y);
                const f2 n = (f2){bflo(dp), bfhi(dp)} * rinv;
                acc = dot2(pkbf(n.x, n.y), wv[e >> 2][e & 3], acc);
            }
            const float r = wave_sum(acc);
            const unsigned rb = pkbf(r, r) & 0xffffu;
            const unsigned key = (rb & 0x8000u) ? (~rb & 0xffffu) : (rb | 0x8000u);
            asm volatile("s_mov_b64 exec, 1\n\tds_max_u32 %0, %1\n\ts_mov_b64 exec, -1" :: "v"(ldsb + L_CT + C_CMAX * 4), "v"((key << 16) | (unsigned)(2047 - myrow)) : "memory");
            if (lds_arrive_rtn(lds, C_CN, lane) == 7) {
                const unsigned k2 = *ctl(lds, C_CMAX);
                const unsigned mb = *ctl(lds, C_ST + (((2047 - (k2 & 0xffffu)) >> 2) & 7));
                if (lane == 0) __builtin_amdgcn_raw_buffer_store_b128((v4u){k2, tag, mb, tag}, rws, WS_CAND + b * 16, 0, AUX_SC1);
            }
        }
        STAMP(3);
        lds_wait_ge(lds, C_LN, 8);
        {
            const unsigned m = *ctl(lds, C_ST + (lane & 7));
            const unsigned w1 = (m << 16) | tag16, w3 = (m & 0xffff0000u) | tag16;
#pragma unroll
            for (int i = 0; i < 4; i++) {
                const int cgx = (T >> 3) + 64 * i;
                const v2u x4 = *(const v2u*)(lds + L_U + (lane & 7) * LNS + cgx * 8);
                __builtin_amdgcn_raw_buffer_store_b128((v4u){x4.x, w1, x4.y, w3}, rws, WS_XT + (cgx * 256 + ((b + cgx) & 255)) * 128 + (lane & 7) * 16, 0, AUX_SC1);
            }
        }
        *ctl(lds, C_TAG) = tag;
        lds_arrive(lds, C_SI, lane);
        __builtin_amdgcn_s_setprio(1);
        STAMP(4);
        const int g4s = ((g4 & 1) << 1) | (g4 >> 1);
        if (hw < 7) {
            const int v = 8 * ((hw == 3 || hw == 5) ? g4s : g4) + l15;
            const int o = (hw == 0 ? 18 : hw == 1 ? 21 : hw == 2 ? 26 : hw == 3 ? 21 : hw == 4 ? 20 : hw == 5 ? 19 : 18);
            const int w = hw == 0 ? 3 : hw == 1 ? 5 : hw == 2 ? 3 : hw == 3 ? 5 : hw == 4 ? 7 : hw == 5 ? 9 : 2;
            *(v4u*)(lds + L_BD + hw * 1024 + lane * 16) = band_frag(v - o, w, hw == 6 ? 0xffffu : 0x3C00u);
        }
        lds_arrive(lds, C_BD, lane);
#define POLL_DELAY 8
        __builtin_amdgcn_s_sleep(POLL_DELAY);
        v4u gv[4];
        {
            const int G = 8 * hw + (lane >> 3);
            _Pragma("nounroll") for (unsigned s = 0; s < SPIN_CAP; s++) {
#pragma unroll
                for (int j = 0; j < 4; j++) gv[j] = __builtin_amdgcn_raw_buffer_load_b128(rws, WS_XT + (cg * 256 + ((4 * G + j + cg) & 255)) * 128 + (lane & 7) * 16, 0, AUX_SC1);
                unsigned bad = 0;
#pragma unroll
                for (int j = 0; j < 4; j++) bad |= (gv[j].y ^ tag16) | (gv[j].w ^ tag16);
                if (__all((bad & 0xffffu) == 0)) break;
                __builtin_amdgcn_s_sleep(4);
            }
        }
        __builtin_amdgcn_s_setprio(3);
        STAMP(5);
        float base[4][4];
        {
            const float d1[4] = {(1.0f + bflo(dsk2.x)) * (1.0f / 128), (1.0f + bfhi(dsk2.x)) * (1.0f / 128), (1.0f + bflo(dsk2.y)) * (1.0f / 128), (1.0f + bfhi(dsk2.y)) * (1.0f / 128)};
            float un[4][4];
#pragma unroll
            for (int r = 0; r < 4; r++) {
                const float m = bfhi(gv[r].y), rinv = __builtin_amdgcn_rcpf(bfhi(gv[r].w)) * 128.0f;
#pragma unroll
                for (int p = 0; p < 2; p++) {
                    const unsigned xw = p ? gv[r].z : gv[r].x;
                    const f2 xx = {bflo(xw), bfhi(xw)};
                    const f2 d = xx - m;
                    const unsigned dp = pkbf(d.x, d.y);
                    const f2 n = (f2){bflo(dp), bfhi(dp)} * rinv;
                    const unsigned np = pkbf(n.x, n.y);
                    const f2 u = {bflo(np), bfhi(np)};
                    const f2 bs = __builtin_elementwise_fma(u, (f2){d1[2 * p], d1[2 * p + 1]}, xx);
                    un[r][2 * p] = u.x; un[r][2 * p + 1] = u.y; base[r][2 * p] = bs.x; base[r][2 * p + 1] = bs.y;
                }
            }
            const int off = uslot((T >> 1) + 68) * 16 + ((T & 1) ? 0 : 8);
            lds_wait_ge(lds, C_SI, 8);
#pragma unroll
            for (int c = 0; c < 4; c++) *(v2u*)(lds + L_U + c * UB + off) = (v2u){pkh(un[3][c], un[2][c]), pkh(un[1][c], un[0][c])};
            if (l31 < 17) *(v4u*)(lds + L_U + (T >> 7) * UB + (uslot((T >> 5) & 3) + l31) * 16) = (v4u){0, 0, 0, 0};
        }
        lds_arrive(lds, C_U, lane);
        STAMP(6);
        if (hw >= 4) {
            const int c = hw - 4;
            char* const U = lds + L_U + c * UB;
            char* const R = lds + L_XY + c * XYB;
            lds_wait_ge(lds, C_T + c, 1);
            const int e4 = 79 - l15 + 4 * g4, e8 = 127 - l15 + 4 * g4;
            const char* const f4b = R + O_FT + (e4 & 1) * 192 + (e4 >> 1) * 4;
            const char* const f8b = R + O_FT + (e8 & 1) * 192 + (e8 >> 1) * 4;
            auto ldTf = [&](const char* bp, int ka, int d) {
                const unsigned* pa_ = (const unsigned*)(bp + ka * 384 - 32 * d); const unsigned* pb_ = (const unsigned*)(bp + (ka + 1) * 384 - 32 * d);
                return (v4u){pa_[0], pa_[1], pb_[0], pb_[1]};
            };
            lds_wait_ge(lds, C_BD, 8);
            v4u bd[4];
#pragma unroll
            for (int k = 0; k < 4; k++) bd[k] = *(const v4u*)(lds + L_BD + k * 1024 + lane * 16);
            v4u t01[3], t23[3];
#pragma unroll
            for (int d = 0; d < 3; d++) { t01[d] = ldTf(f4b, 0, d + 2); t23[d] = ldTf(f4b, 2, d + 2); }
            lds_wait_ge(lds, C_U, 8);
            STAMP(7);
            const char* const uap = U + uslot(2 * l15 + 1 - g4 + 68) * 16;
            const char* const ubp = U + uslot(2 * l15 - g4s + 68) * 16;
            const f4 z4 = {0, 0, 0, 0};
            auto pkW = [&](f4 a, f4 b_) { return (v4u){pkh(a[0], a[1]), pkh(a[2], a[3]), pkh(b_[0], b_[1]), pkh(b_[2], b_[3])}; };
            f4 F[8];
#pragma unroll
            for (int t = 2; t < 8; t++) F[t] = z4;
            const int s0 = 4 * l15 + g4;
            char* const fw = R + O_YF + (s0 + (s0 >> 3)) * 16;
#pragma unroll
            for (int tp = 0; tp < 6; tp++) {
                const v4u ua = *(const v4u*)(uap + tp * 128), ub = *(const v4u*)(ubp + tp * 128);
                const v4u w01 = pkW(MFMA16(as_h8(ua), as_h8(bd[0]), z4), MFMA16(as_h8(ua), as_h8(bd[1]), z4));
                const v4u w23 = pkW(MFMA16(as_h8(ua), as_h8(bd[2]), z4), MFMA16(as_h8(ub), as_h8(bd[3]), z4));
#pragma unroll
                for (int d = 2; d <= 4; d++) if (tp + d < 8) {
                    F[tp + d] = MFMA16(as_h8(w01), as_h8(t01[d - 2]), F[tp + d]);
                    F[tp + d] = MFMA16(as_h8(w23), as_h8(t23[d - 2]), F[tp + d]);
                }
                if (tp == 1) {
                    *(f4*)(fw + 72 * 16 * 2) = F[2]; *(f4*)(fw + 72 * 16 * 3) = F[3];
                    lds_arrive(lds, C_Y0, lane);
                    __builtin_amdgcn_s_setprio(1);
                }
            }
            __builtin_amdgcn_sched_barrier(0);
            {
                v4u t45[4];
#pragma unroll
                for (int d = 0; d < 4; d++) t45[d] = ldTf(f8b, 4, d + 4);
                const v4u b4 = *(const v4u*)(lds + L_BD + 4096 + lane * 16), b5 = *(const v4u*)(lds + L_BD + 5120 + lane * 16);
                const v4u an = *(const v4u*)(lds + L_BD + 6144 + lane * 16) & *ctl(lds, C_K1024 + c);
#pragma unroll
                for (int tp = 0; tp < 4; tp++) {
                    const v4u ua = *(const v4u*)(uap + tp * 128), ub = *(const v4u*)(ubp + tp * 128);
                    const v4u w45 = pkW(MFMA16(as_h8(ua), as_h8(b4), z4), MFMA16(as_h8(ub), as_h8(b5), z4));
                    F[tp + 4] = MFMA16(as_h8(an), as_h8(ua), F[tp + 4]);
#pragma unroll
                    for (int d = 4; d <= 7; d++) if (tp + d < 8) F[tp + d] = MFMA16(as_h8(w45), as_h8(t45[d - 4]), F[tp + d]);
                }
            }
#pragma unroll
            for (int t = 4; t < 8; t++) *(f4*)(fw + 72 * 16 * t) = F[t];
            lds_arrive(lds, C_Y1, lane);
            STAMP(8);
        }
        const unsigned idx = 2047 - (lds_wait_ge(lds, C_IDX, 1) & 2047u);
        const f4 osc = *(const f4*)ctl(lds, C_OSC);
        __builtin_amdgcn_s_setprio(3);
        lds_wait_ge(lds, hw >= 4 ? C_Y1 : C_Y0, 8);
        STAMP(9);
        __builtin_amdgcn_s_setprio(0);
        {
            const char* const yp = lds + L_XY + (T + (T >> 3)) * 16;
            f4 yv[4];
#pragma unroll
            for (int c = 0; c < 4; c++) yv[c] = *(const f4*)(yp + c * XYB + O_YD);
            if (hw >= 2) {
#pragma unroll
                for (int c = 0; c < 4; c++) yv[c] += *(const f4*)(yp + c * XYB + O_YF);
            }
#pragma unroll
            for (int r = 0; r < 4; r++) {
                const int row = 4 * T + r;
                const float o0 = fmaf(yv[0][r], osc[0], base[r][0]), o1 = fmaf(yv[1][r], osc[1], base[r][1]), o2 = fmaf(yv[2][r], osc[2], base[r][2]), o3 = fmaf(yv[3][r], osc[3], base[r][3]);
                __builtin_amdgcn_raw_buffer_store_b64((v2u){pkbf(o0, o1), pkbf(o2, o3)}, rout, row == (int)idx ? 0x7ffffff0 : row * 2048 + cg * 8, 0, 0);
                if (row == (int)idx) {
                    *(f4*)ctl(lds, C_ROWV) = (f4){o0, o1, o2, o3};
                    asm volatile("s_waitcnt lgkmcnt(0)" ::: "memory");
                    __hip_atomic_store(ctl(lds, C_ROWF), 1u, __ATOMIC_RELAXED, __HIP_MEMORY_SCOPE_WORKGROUP);
                }
            }
        }
        STAMP(10);
        SWAITVM;
        STAMP(11);
        SFLUSH(A.ws);
        __builtin_amdgcn_endpgm();
    } else if (wave2 < 4) {
        __builtin_amdgcn_s_setprio(2);
        asm volatile("" : "+v"(pl), "+v"(pa), "+v"(pcr), "+v"(pci), "+v"(pd));
        const float ldt = (wave & 1) ? bfhi(pd) : bflo(pd);
        STAMP(1);
        char* const R = lds + L_XY + wave * XYB;
        {
            const float dt = rbf(fexp(ldt));
            float ar2[2], air[2];
            ar2[0] = (-rbf(fexp(bflo(pl))) * dt) * LOG2E; ar2[1] = (-rbf(fexp(bfhi(pl))) * dt) * LOG2E;
            air[0] = (bflo(pa) * dt) * INV2PI; air[1] = (bfhi(pa) * dt) * INV2PI;
            float mx = fmaxf(fabsf(bflo(pcr)) + fabsf(bflo(pci)), fabsf(bfhi(pcr)) + fabsf(bfhi(pci))) * (2.0f * dt);
            mx = wave_max(mx);
            int E = (__float_as_uint(mx) >> 23) & 0xff; E = min(max(E, 20), 240);
            const float cs = __uint_as_float((unsigned)(262 - E) << 23);
            if (lane == 0) *ctl(lds, C_OSC + wave) = (unsigned)(E - 14) << 23;
            f2 C[2] = {(f2){bflo(pcr) * dt * cs, -(bflo(pci) * dt * cs)}, (f2){bfhi(pcr) * dt * cs, -(bfhi(pci) * dt * cs)}};
            const unsigned xb = ldsb + L_XY + wave * XYB + (lane & 1) * 8;
            const int sl = lane >> 1;
            auto putX = [&](int row, const f2* a) { ldsst<v2u>(xb + ((sl ^ (row & 15)) << 4), row * 512, (v2u){pkh(a[0].x, a[0].y), pkh(a[1].x, a[1].y)}); };
            auto mul2 = [&](f2* o, const f2* a, const f2* m) { o[0] = cmul(a[0], m[0]); o[1] = cmul(a[1], m[1]); };
            f2 z[2], zb[2], z2[2], t[2], p256[2], p258[2], p260[2], p262[2], p512[2], p516[2], p772[2];
#pragma unroll
            for (int m = 0; m < 2; m++) { z[m] = cexp_p(ar2[m], -air[m], 1.0f); zb[m] = cexp_p(ar2[m], -air[m], 256.0f); z2[m] = cmul(z[m], z[m]); t[m] = C[m]; }
#pragma unroll
            for (int e = 0; e < 8; e++) { putX(e, t); if (e < 7) mul2(t, t, z); }
            mul2(p256, C, zb); mul2(p258, p256, z2); mul2(p260, p258, z2); mul2(p262, p260, z2);
            putX(8, p256); putX(9, p258); putX(10, p260); putX(11, p262);
            mul2(p512, p256, zb); mul2(p516, p260, zb); mul2(t, p512, zb); mul2(p772, p516, zb);
            putX(12, p512); putX(13, p516); putX(14, t); putX(15, p772);
#pragma unroll
            for (int e = 0; e < 5; e++) { mul2(t, t, zb); putX(16 + e, t); }
            STAMP(2);
            ygen(ar2, air, 128.0f, ldsb + L_XY + wave * XYB + 37 * 512, lane);
        }
        const int o1 = 64 + 16 * l31 + 8 * hh;
        int ad[4];
        {
            const int bo = l31 & 1;
            int a0, a1, st2;
            if (hh) { a0 = (bo ? 1 : 3) * 384 + 2 * (79 - (31 + ((l31 + 1) >> 1))); a1 = (bo ? 2 : 0) * 384 + 2 * (79 - (32 + (l31 >> 1))); st2 = 32; }
            else { a0 = (bo ? 4 : 5) * 384 + 2 * (127 - (63 + ((l31 + 1) >> 1))); a1 = a0 - 32; st2 = 64; }
            ad[0] = a0; ad[1] = a1; ad[2] = a0 - st2; ad[3] = a1 - st2;
            if (lane == 0) ad[0] = 3 * 384 + 2 * 16;
            if (lane == 32) ad[0] = 3 * 384 + 2 * 70;
#pragma unroll
            for (int e = 0; e < 4; e++) ad[e] = opq(ad[e] + ldsb + L_XY + wave * XYB + O_FT);
        }
        STAMP(3);
        lds_wait_ge(lds, C_X + wave, 1);
        STAMP(4);
        f16v kacc;
#pragma unroll
        for (int e = 0; e < 16; e++) kacc[e] = 0.0f;
        {
            const int lp = hh ^ (l31 & 15);
            v4u xa[16], yb[16];
#pragma unroll
            for (int t = 0; t < 16; t++) {
                const int so = ((lp ^ ((2 * t) & 15)) + ((2 * t) & 16)) << 4;
                xa[t] = *(const v4u*)(R + l31 * 512 + so);
                yb[t] = *(const v4u*)(R + (21 + l31) * 512 + so);
                if (t >= 4) kacc = MFMA32(as_h8(xa[t - 4]), as_h8(yb[t - 4]), kacc);
                __builtin_amdgcn_sched_barrier(0);
            }
            const v4u z = {0, 0, 0, 0};
            *(v4u*)(R + O_FT + lane * 16) = z; *(v4u*)(R + O_FT + 1024 + lane * 16) = z; if (lane < 16) *(v4u*)(R + O_FT + 2048 + lane * 16) = z;
            { const int cp = lane >> 4, o = lane & 15; *(v4u*)(R + O_KD + cp * 1344 + (o < 5 ? o * 16 : 1072 + (o - 5) * 16)) = z; }
#pragma unroll
            for (int t = 12; t < 16; t++) kacc = MFMA32(as_h8(xa[t]), as_h8(yb[t]), kacc);
        }
        STAMP(5);
        {
            char* const KD = R + O_KD;
            char* const FT = R + O_FT;
            float kv[12];
#pragma unroll
            for (int e = 0; e < 12; e++) kv[e] = kacc[e];
            const f2 sw = swap32(kv[0], kv[0]);
            float n0 = hh ? dppf<0x130>(sw.x) : sw.y;
            const float k256 = __uint_as_float(__builtin_amdgcn_readlane(__float_as_uint(kv[4]), 0));
            n0 = lane == 63 ? k256 : n0;
            const unsigned p01 = pkh(kv[0], kv[1]), p23 = pkh(kv[2], kv[3]), p12 = pkh(kv[1], kv[2]), p3n = pkh(kv[3], n0);
            *(v2u*)(KD + o1) = (v2u){p01, p23};
            *(v2u*)(KD + KD1 + o1) = (v2u){p12, p3n};
            *(unsigned*)(KD + KD2 + o1 - 4) = p01; *(unsigned*)(KD + KD2 + o1) = p23;
            *(unsigned*)(KD + KD3 + o1 - 4) = p12; *(unsigned*)(KD + KD3 + o1) = p3n;
            const float vn = dppf<0x130>(kv[4]);
            const unsigned q00 = pkh(kv[4], kv[4]), q22 = pkh(kv[6], kv[6]);
            if (hh == 0) {
                const v4u ev = {q00, pkh(kv[5], kv[6]), q22, pkh(kv[7], vn)};
                const v4u od = {pkh(kv[4], kv[5]), q22, pkh(kv[6], kv[7]), pkh(vn, vn)};
                const int o2 = 576 + 16 * l31;
                *(v4u*)(KD + o2) = ev;
                *(v2u*)(KD + KD1 + o2) = (v2u){od[0], od[1]}; *(v2u*)(KD + KD1 + o2 + 8) = (v2u){od[2], od[3]};
                unsigned* const e2 = (unsigned*)(KD + KD2 + o2 - 4); unsigned* const o3 = (unsigned*)(KD + KD3 + o2 - 4);
#pragma unroll
                for (int k = 0; k < 4; k++) { e2[k] = ev[k]; o3[k] = od[k]; }
            }
            if (lane == 0) { *(u16*)(KD + KD1 + 62) = (u16)p01; *(u16*)(KD + KD3 + 58) = (u16)p01; *ctl(lds, C_K1024 + wave) = pkh(kv[8], kv[8]); }
            if (lane == 32) {
                const unsigned q0z = q00 & 0xffffu;
                *(v2u*)(KD + 1088) = (v2u){q00, q0z};
                *(unsigned*)(KD + KD2 + 1084) = q00; *(unsigned*)(KD + KD2 + 1088) = q0z;
                *(unsigned*)(KD + KD1 + 1088) = q00; *(unsigned*)(KD + KD3 + 1084) = q00;
                *(u16*)(FT + 5 * 384) = (u16)pkh(kv[8], kv[8]);
            }
#pragma unroll
            for (int e = 0; e < 4; e++) {
                const u16 hv = (u16)pkh(hh ? kv[4 + e] : kv[8 + e], 0.0f);
                ldsst<u16>(ad[e], 0, hv); ldsst<u16>(ad[e], 190, hv);
            }
        }
        lds_arrive(lds, C_T + wave, lane);
        STAMP(6);
        const int rR = 16 * ((l31 >> 2) & 1) + 4 * (l31 >> 3) + (l31 & 3);
        const int tau = rR + 8 * hh + 1;
        const unsigned ka0 = opq(ldsb + L_XY + wave * XYB + O_KD + (tau & 3) * 1344 + ((tau & 3) ? 8 : 0) + (tau >> 2) * 8);
        const unsigned ka1 = opq(ka0 + 8);
        unsigned ub = ldsb + L_U + wave * UB + (l31 + 96 - 96 * hh) * 16;
        unsigned yw = ldsb + L_XY + wave * XYB + O_YD + (9 * l31 + 4 * hh) * 16;
        constexpr int PD = 4;
        v2u kf0[35], kf1[35];
#pragma unroll
        for (int j = 0; j < PD; j++) { kf0[j] = ldsld<v2u>(ka0, 32 * j); kf1[j] = ldsld<v2u>(ka1, 32 * j); }
        lds_wait_ge(lds, C_U, 8);
        __builtin_amdgcn_s_setprio(2);
        STAMP(7);
#pragma unroll 1
        for (int be = 0; be < 2; be++) {
            auto uoff = [](int j) { const int t = 3 - 2 * j; const int fl = (t >= 0) ? (t >> 2) : -((-t + 3) >> 2); return (uplane(t & 3) - 96 + 17 + fl) * 16; };
            v4u uf[35];
#pragma unroll
            for (int j = 0; j < PD; j++) uf[j] = ldsld<v4u>(ub, uoff(j));
            f16v acc;
#pragma unroll
            for (int e = 0; e < 16; e++) acc[e] = 0.0f;
            __builtin_amdgcn_sched_barrier(0);
#pragma unroll
            for (int j = 0; j < 35; j++) {
                acc = MFMA32(as_h8((v4u){kf0[j].x, kf0[j].y, kf1[j].x, kf1[j].y}), as_h8(uf[j]), acc);
                const int jn = (j + PD) % 35;
                kf0[jn] = ldsld<v2u>(ka0, 32 * jn); kf1[jn] = ldsld<v2u>(ka1, 32 * jn);
                if (j + PD < 35) uf[j + PD] = ldsld<v4u>(ub, uoff(j + PD));
                __builtin_amdgcn_sched_barrier(0);
            }
#pragma unroll
            for (int g = 0; g < 4; g++) ldsst<f4>(yw, g * 16, (f4){acc[4 * g], acc[4 * g + 1], acc[4 * g + 2], acc[4 * g + 3]});
            lds_arrive(lds, C_Y0 + be, lane);
            __builtin_amdgcn_s_setprio(1);
            ub += 32 * 16; yw += 288 * 16;
            if (be == 0) STAMP(8); else STAMP(9);
        }
        asm volatile("" :: "v"(pf));
        SFLUSH(A.ws);
        __builtin_amdgcn_endpgm();
    } else {
        const int wv = wave - 4;
        const bool moe = (b >> 3) < 28;
        const int mo = (b & 7) * 28 + (b >> 3), fb = mo >> 2, kq = mo & 3;
        const int k0 = kq * 256 + wv * 64 + (lane >> 3) * 8;
        v4u wgv[8], wuv[8];
        auto rg = mkrs(A.wg, moe ? DM * FF * 2 : 0), ru = mkrs(A.wu, moe ? DM * FF * 2 : 0);
#define W1N 4
#pragma unroll
        for (int i = 0; i < W1N; i++) {
            wgv[i] = __builtin_amdgcn_raw_buffer_load_b128(rg, (k0 + i) * 7168 + fb * 128 + (lane & 7) * 16, 0, AUX_W);
            wuv[i] = __builtin_amdgcn_raw_buffer_load_b128(ru, (k0 + i) * 7168 + fb * 128 + (lane & 7) * 16, 0, AUX_W);
        }
        __builtin_amdgcn_sched_barrier(0);
        asm volatile("" : "+v"(pl), "+v"(pa), "+v"(pd), "+v"(pf));
        __builtin_amdgcn_s_setprio(2);
        STAMP(1);
        {
            const float dt = rbf(fexp((wave & 1) ? bfhi(pd) : bflo(pd)));
            float ar2[2], air[2];
            ar2[0] = (-rbf(fexp(bflo(pl))) * dt) * LOG2E; ar2[1] = (-rbf(fexp(bfhi(pl))) * dt) * LOG2E;
            air[0] = (bflo(pa) * dt) * INV2PI; air[1] = (bfhi(pa) * dt) * INV2PI;
            ygen(ar2, air, 0.0f, ldsb + L_XY + wv * XYB + 21 * 512, lane);
        }
        lds_arrive(lds, C_X + wv, lane);
        __builtin_amdgcn_s_setprio(0);
        STAMP(2);
        if (!moe && wv != 0) { SFLUSH(A.ws); __builtin_amdgcn_endpgm(); }
        lds_wait_ge(lds, C_SI, 8);
        const unsigned tag = *ctl(lds, C_TAG);
#pragma unroll
        for (int i = W1N; i < 8; i++) {
            wgv[i] = __builtin_amdgcn_raw_buffer_load_b128(rg, (k0 + i) * 7168 + fb * 128 + (lane & 7) * 16, 0, AUX_W);
            wuv[i] = __builtin_amdgcn_raw_buffer_load_b128(ru, (k0 + i) * 7168 + fb * 128 + (lane & 7) * 16, 0, AUX_W);
        }
        __builtin_amdgcn_sched_barrier(0);
        if (wv == 0) {
            v4u cn[4];
            _Pragma("nounroll") for (unsigned s = 0; s < SPIN_CAP; s++) {
#pragma unroll
                for (int j = 0; j < 4; j++) cn[j] = __builtin_amdgcn_raw_buffer_load_b128(rws, WS_CAND + (lane + 64 * j) * 16, 0, AUX_SC1);
                unsigned bad = 0;
#pragma unroll
                for (int j = 0; j < 4; j++) bad |= (cn[j].y ^ tag) | (cn[j].w ^ tag);
                if (__all(bad == 0)) break;
                __builtin_amdgcn_s_sleep(2);
            }
            __builtin_amdgcn_s_setprio(3);
            const unsigned mk = max(max(cn[0].x, cn[1].x), max(cn[2].x, cn[3].x));
            const unsigned bst = wave_umax(mk);
            if (mk == bst) {
                *ctl(lds, C_MS) = cn[0].x == bst ? cn[0].z : cn[1].x == bst ? cn[1].z : cn[2].x == bst ? cn[2].z : cn[3].z;
                *ctl(lds, C_IDX) = bst | 0x8000u;
                if (b == 0) __builtin_amdgcn_raw_buffer_store_b32(tag, rws, WS_EPOCH, 0, AUX_SC1);
            }
        }
        const unsigned best = lds_wait_ge(lds, C_IDX, 1);
        __builtin_amdgcn_s_setprio(3);
        STAMP(3);
        const unsigned idx = 2047 - (best & 2047u);
        const unsigned key = best >> 16;
        const float rsel = bflo((key & 0x8000u) ? (key & 0x7fffu) : (~key & 0xffffu));
        bool fin = true;
        if (moe) {
            auto rx = mkrs(A.x, LSEQ * DM * 2);
            const v4u xs = __builtin_amdgcn_raw_buffer_load_b128(rx, idx * 2048 + k0 * 2, 0, 0);
            v2u wdv[16];
            auto rd = mkrs(A.wd, DM * FF * 2);
#pragma unroll
            for (int i = 0; i < WD1; i++) wdv[i] = __builtin_amdgcn_raw_buffer_load_b64(rd, (fb * 64 + wv * 16 + i) * 2048 + kq * 512 + lane * 8, 0, AUX_W);
            __builtin_amdgcn_sched_barrier(0);
            const unsigned ms = *ctl(lds, C_MS);
            const float m = bflo(ms), rinv = __builtin_amdgcn_rcpf(bfhi(ms));
            float acc[16];
#pragma unroll
            for (int c = 0; c < 16; c++) acc[c] = 0;
#pragma unroll
            for (int i2 = 0; i2 < 4; i2++) {
                const unsigned dp = pkbf(bflo(xs[i2]) - m, bfhi(xs[i2]) - m);
                const unsigned np = pkbf(bflo(dp) * rinv, bfhi(dp) * rinv);
                const unsigned nlo = np & 0xffffu, nlo1 = np << 16, nhi = np >> 16, nhi1 = np & 0xffff0000u;
#pragma unroll
                for (int s = 0; s < 2; s++) {
                    const int i = 2 * i2 + s;
                    const unsigned xl = s ? nhi : nlo, xh = s ? nhi1 : nlo1;
#pragma unroll
                    for (int c = 0; c < 4; c++) {
                        acc[2 * c] = dot2(wgv[i][c], xl, acc[2 * c]);
                        acc[2 * c + 1] = dot2(wgv[i][c], xh, acc[2 * c + 1]);
                        acc[8 + 2 * c] = dot2(wuv[i][c], xl, acc[8 + 2 * c]);
                        acc[8 + 2 * c + 1] = dot2(wuv[i][c], xh, acc[8 + 2 * c + 1]);
                    }
                }
            }
            __builtin_amdgcn_sched_barrier(0);
#pragma unroll
            for (int i = WD1; i < 16; i++) wdv[i] = __builtin_amdgcn_raw_buffer_load_b64(rd, (fb * 64 + wv * 16 + i) * 2048 + kq * 512 + lane * 8, 0, AUX_W);
            __builtin_amdgcn_sched_barrier(0);
            STAMP(4);
            float r8[8], r4[4], r2[2];
#pragma unroll
            for (int c = 0; c < 8; c++) { f2 t = swap32(acc[c], acc[8 + c]); r8[c] = t.x + t.y; }
#pragma unroll
            for (int c = 0; c < 4; c++) { f2 t = swap16(r8[c], r8[4 + c]); r4[c] = t.x + t.y; }
            const bool b3 = lane & 8;
#pragma unroll
            for (int c = 0; c < 2; c++) { const float keep = b3 ? r4[2 + c] : r4[c], give = b3 ? r4[c] : r4[2 + c]; r2[c] = keep + dppf<0x128>(give); }
            const int vi = (lane >> 5) * 64 + 8 * (lane & 7) + 4 * ((lane >> 4) & 1) + 2 * ((lane >> 3) & 1);
            *(f2*)(lds + L_GUR + wv * 512 + vi * 4) = (f2){r2[0], r2[1]};
            if (lds_arrive_rtn(lds, C_GU, lane) == 3) {
                const f2 s0 = *(const f2*)(lds + L_GUR + lane * 8), s1 = *(const f2*)(lds + L_GUR + 512 + lane * 8), s2 = *(const f2*)(lds + L_GUR + 1024 + lane * 8), s3 = *(const f2*)(lds + L_GUR + 1536 + lane * 8);
                const f2 s = (s0 + s1) + (s2 + s3);
                __builtin_amdgcn_raw_buffer_store_b128((v4u){__float_as_uint(s.x), tag, __float_as_uint(s.y), tag}, rws, WS_GU + mo * 1024 + lane * 16, 0, AUX_SC1);
            }
            STAMP(5);
            v2u gq, uq;
            _Pragma("nounroll") for (unsigned s = 0; s < SPIN_CAP; s++) {
                gq = __builtin_amdgcn_raw_buffer_load_b64(rws, WS_GU + (fb * 4 + g4) * 1024 + (16 * wv + l15) * 8, 0, AUX_SC1);
                uq = __builtin_amdgcn_raw_buffer_load_b64(rws, WS_GU + (fb * 4 + g4) * 1024 + 512 + (16 * wv + l15) * 8, 0, AUX_SC1);
                if (__all(((gq.y ^ tag) | (uq.y ^ tag)) == 0)) break;
                __builtin_amdgcn_s_sleep(2);
            }
            STAMP(6);
            const float gt = rbf(sum32(sum16(__uint_as_float(gq.x)))), ut = rbf(sum32(sum16(__uint_as_float(uq.x))));
            const float hv = rbf(rbf(gt * __builtin_amdgcn_rcpf(1.0f + fexp(-gt))) * ut);
            const unsigned hb = __float_as_uint(hv);
            float p4[4] = {0, 0, 0, 0};
#pragma unroll
            for (int i = 0; i < 16; i++) {
                const unsigned hs = __builtin_amdgcn_readlane(hb, i);
                const unsigned hh1 = hs, hl = hs >> 16;
                p4[0] = dot2(wdv[i].x, hl, p4[0]);
                p4[1] = dot2(wdv[i].x, hh1, p4[1]);
                p4[2] = dot2(wdv[i].y, hl, p4[2]);
                p4[3] = dot2(wdv[i].y, hh1, p4[3]);
            }
            *(f4*)(lds + L_PR + wv * 1024 + lane * 16) = (f4){p4[0], p4[1], p4[2], p4[3]};
            fin = false;
            if (lds_arrive_rtn(lds, C_P, lane) == 3) {
                const f4 s0 = *(const f4*)(lds + L_PR + lane * 16), s1 = *(const f4*)(lds + L_PR + 1024 + lane * 16), s2 = *(const f4*)(lds + L_PR + 2048 + lane * 16), s3 = *(const f4*)(lds + L_PR + 3072 + lane * 16);
                const f4 s = (s0 + s1) + (s2 + s3);
                __builtin_amdgcn_raw_buffer_store_b128((v4u){__float_as_uint(s[0]), tag, __float_as_uint(s[1]), tag}, rws, WS_P + (kq * 64 + lane) * 2048 + fb * 32, 0, AUX_SC1);
                __builtin_amdgcn_raw_buffer_store_b128((v4u){__float_as_uint(s[2]), tag, __float_as_uint(s[3]), tag}, rws, WS_P + (kq * 64 + lane) * 2048 + fb * 32 + 16, 0, AUX_SC1);
                fin = true;
            }
            STAMP(7);
        }
        if (fin) {
            v4u p0, p1;
            const bool on = lane < 48;
            _Pragma("nounroll") for (unsigned s = 0; s < SPIN_CAP; s++) {
                p0 = __builtin_amdgcn_raw_buffer_load_b128(rws, WS_P + cg * 2048 + lane * 16, 0, AUX_SC1);
                p1 = __builtin_amdgcn_raw_buffer_load_b128(rws, WS_P + cg * 2048 + (on ? 1024 : 0) + lane * 16, 0, AUX_SC1);
                if (__all(((p0.y ^ tag) | (p0.w ^ tag) | (p1.y ^ tag) | (p1.w ^ tag)) == 0)) break;
                __builtin_amdgcn_s_sleep(H3_SLEEP);
            }
            STAMP(8);
            float ya = __uint_as_float(p0.x) + (on ? __uint_as_float(p1.x) : 0.0f), yb = __uint_as_float(p0.z) + (on ? __uint_as_float(p1.z) : 0.0f);
            ya += dppf<0x122>(ya); ya += dppf<0x124>(ya); ya += dppf<0x128>(ya); ya = sum32(sum16(ya));
            yb += dppf<0x122>(yb); yb += dppf<0x124>(yb); yb += dppf<0x128>(yb); yb = sum32(sum16(yb));
            const float y0 = ya, y1 = yb, y2 = dppf<0x130>(ya), y3 = dppf<0x130>(yb);
            const float rs = rbf(__builtin_amdgcn_rcpf(1.0f + fexp(-rsel)));
            lds_wait_ge(lds, C_ROWF, 1);
            const f4 rv = *(const f4*)ctl(lds, C_ROWV);
            const float o0 = rv[0] + rbf(rbf(y0) * rs), o1 = rv[1] + rbf(rbf(y1) * rs), o2 = rv[2] + rbf(rbf(y2) * rs), o3 = rv[3] + rbf(rbf(y3) * rs);
            if (lane == 0) __builtin_amdgcn_raw_buffer_store_b64((v2u){pkbf(o0, o1), pkbf(o2, o3)}, rout, idx * 2048 + cg * 8, 0, 0);
            STAMP(9);
        }
        SFLUSH(A.ws);
    }
}

extern "C" void kernel_launch(void* const* d_in, const int* in_sizes, int n_in,
                              void* d_out, int out_size, void* d_ws, size_t ws_size,
                              hipStream_t stream) {
    KArgs a;
    a.x = (const u16*)d_in[0]; a.lar = (const u16*)d_in[1]; a.aim = (const u16*)d_in[2]; a.cre = (const u16*)d_in[3]; a.cim = (const u16*)d_in[4];
    a.ldt = (const u16*)d_in[5]; a.dsk = (const u16*)d_in[6]; a.wr = (const u16*)d_in[8];
    a.wg = (const u16*)d_in[9]; a.wu = (const u16*)d_in[10]; a.wd = (const u16*)d_in[11];
    a.out = (u16*)d_out; a.ws = (char*)d_ws;
    k_main<<<256, 1024, 0, stream>>>(a);
}
```

```cpp
#include <hip/hip_runtime.h>
#include <stdint.h>

typedef unsigned short u16;
typedef unsigned v4u __attribute__((ext_vector_type(4)));
typedef unsigned v2u __attribute__((ext_vector_type(2)));
typedef float f4 __attribute__((ext_vector_type(4)));
typedef float f2 __attribute__((ext_vector_type(2)));
typedef float f16v __attribute__((ext_vector_type(16)));
typedef _Float16 h8 __attribute__((ext_vector_type(8)));
typedef _Float16 h2 __attribute__((ext_vector_type(2)));
typedef __bf16 b2 __attribute__((ext_vector_type(2)));
#define DI __device__ __forceinline__
#define LAS __attribute__((address_space(3)))
#define AUX_NT 2
#define AUX_SC1 16
constexpr int LSEQ = 2048, DM = 1024, FF = 3584;
constexpr unsigned WS_EPOCH = 0;
constexpr unsigned WS_CAND = 0xC0000;
constexpr unsigned WS_REC = 0xD0000;
constexpr unsigned WS_GU = 16384;
constexpr unsigned WS_P = WS_GU + 224 * 1024;
constexpr unsigned WS_XT = 1u << 20;
constexpr unsigned WS_BYTES = WS_XT + (4u << 20);
constexpr unsigned CODE_PF = 19456;
constexpr unsigned CAND_T = 5500, FL_T = 8000, H3_T = 7000;
constexpr unsigned SPIN_CAP = 1u << 16;

DI float bflo(unsigned v) { return __uint_as_float(v << 16); }
DI float bfhi(unsigned v) { return __uint_as_float(v & 0xffff0000u); }
DI unsigned pkbf(float a, float b) { b2 v = {(__bf16)a, (__bf16)b}; return __builtin_bit_cast(unsigned, v); }
DI b2 asb2(unsigned v) { return __builtin_bit_cast(b2, v); }
DI float dot2(unsigned a, unsigned b, float c) { return __builtin_amdgcn_fdot2_f32_bf16(asb2(a), asb2(b), c, false); }
DI float rbf(float a) { return bflo(pkbf(a, a)); }
DI unsigned pkh(float a, float b) { h2 v = {(_Float16)a, (_Float16)b}; return __builtin_bit_cast(unsigned, v); }
template <int C> DI float dppf(float x) { return __int_as_float(__builtin_amdgcn_update_dpp(0, __float_as_int(x), C, 0xf, 0xf, true)); }
template <int C> DI unsigned dppu(unsigned x) { return (unsigned)__builtin_amdgcn_update_dpp(0, (int)x, C, 0xf, 0xf, true); }
DI f2 swap16(float a, float b) { auto r = __builtin_amdgcn_permlane16_swap(__float_as_uint(a), __float_as_uint(b), false, false); return (f2){__uint_as_float(r[0]), __uint_as_float(r[1])}; }
DI f2 swap32(float a, float b) { auto r = __builtin_amdgcn_permlane32_swap(__float_as_uint(a), __float_as_uint(b), false, false); return (f2){__uint_as_float(r[0]), __uint_as_float(r[1])}; }
DI float sum16(float a) { f2 r = swap16(a, a); return r.x + r.y; }
DI float sum32(float a) { f2 r = swap32(a, a); return r.x + r.y; }
DI float wave_sum(float v) { v += dppf<0x121>(v); v += dppf<0x122>(v); v += dppf<0x124>(v); v += dppf<0x128>(v); return sum32(sum16(v)); }
DI float wave_max(float v) {
    v = fmaxf(v, dppf<0x121>(v)); v = fmaxf(v, dppf<0x122>(v)); v = fmaxf(v, dppf<0x124>(v)); v = fmaxf(v, dppf<0x128>(v));
    f2 r = swap16(v, v); v = fmaxf(r.x, r.y); r = swap32(v, v); return fmaxf(r.x, r.y);
}
DI unsigned wave_umax(unsigned v) {
    v = max(v, dppu<0x121>(v)); v = max(v, dppu<0x122>(v)); v = max(v, dppu<0x124>(v)); v = max(v, dppu<0x128>(v));
    auto r = __builtin_amdgcn_permlane16_swap(v, v, false, false); v = max(r[0], r[1]);
    r = __builtin_amdgcn_permlane32_swap(v, v, false, false); return max(r[0], r[1]);
}
DI float fexp(float x) {
    const float t = x * 1.4426950408889634f;
    const float lo = fmaf(x, 1.4426950408889634f, -t) + x * 1.925963033500011e-8f;
    const float e = __builtin_amdgcn_exp2f(t);
    return e * fmaf(lo, 0.6931471805599453f, 1.0f);
}
DI __amdgpu_buffer_rsrc_t mkrs(const void* p, unsigned bytes) { return __builtin_amdgcn_make_buffer_rsrc((void*)p, 0, bytes, 0x00020000); }

constexpr int UB = 6144;
constexpr int L_U = 0;
constexpr int LNS = 2080;
DI constexpr int uplane(int p) { return p == 0 ? 0 : p == 1 ? 96 : p == 2 ? 200 : 296; }
DI int uslot(int q) { return ((q & 1) ? 96 : 0) + ((q & 2) ? 200 : 0) + (q >> 2); }
constexpr int XYB = 53 * 512;
constexpr int L_XY = 4 * UB;
constexpr int O_YD = 0, O_YF = 9216, O_KD = 18432, KD1 = 1352, KD2 = 2696, KD3 = 4040, O_FT = O_KD + 5376;
static_assert(O_FT + 2304 <= XYB, "region");
constexpr int L_GUR = L_XY + 4 * XYB;
constexpr int L_PR = L_GUR + 2048;
constexpr int L_BD = L_PR + 4096;
constexpr int L_CT = L_BD + 7168;
constexpr int L_TOT = L_CT + 256;
enum { C_LN = 0, C_CMAX = 1, C_CN = 2, C_U = 3, C_Y0 = 4, C_Y1 = 5, C_GU = 6, C_P = 7, C_IDX = 8, C_ROWF = 9, C_TAG = 10, C_SI = 11, C_T = 16, C_K1024 = 20,
       C_OSC = 24, C_ROWV = 28, C_ST = 32, C_MS = 40, C_BD = 41, C_WL = 42, C_U1 = 46, C_DR = 48 };

struct KArgs {
    const u16* x; const u16* lar; const u16* aim; const u16* cre; const u16* cim; const u16* ldt; const u16* dsk; const u16* wr;
    const u16* wg; const u16* wu; const u16* wd; u16* out; char* ws;
};

DI unsigned* ctl(char* lds, int w) { return (unsigned*)(lds + L_CT) + w; }
DI void lds_arrive(char* lds, int word, int lane) {
    asm volatile("s_mov_b64 exec, 1\n\tds_add_u32 %0, %1\n\ts_mov_b64 exec, -1" :: "v"((unsigned)(size_t)(LAS unsigned*)ctl(lds, word)), "v"(1u) : "memory");
}
DI unsigned lds_arrive_rtn(char* lds, int word, int lane) {
    unsigned r;
    asm volatile("s_mov_b64 exec, 1\n\tds_add_rtn_u32 %0, %1, %2\n\ts_mov_b64 exec, -1\n\ts_waitcnt lgkmcnt(0)" : "=&v"(r) : "v"((unsigned)(size_t)(LAS unsigned*)ctl(lds, word)), "v"(1u) : "memory");
    return __builtin_amdgcn_readfirstlane(r);
}
DI unsigned lds_wait_ge(char* lds, int word, unsigned want) {
    unsigned v;
    _Pragma("nounroll") for (unsigned s = 0; s < (1u << 22); s++) {
        v = __builtin_amdgcn_readfirstlane(__hip_atomic_load(ctl(lds, word), __ATOMIC_RELAXED, __HIP_MEMORY_SCOPE_WORKGROUP));
        if (v >= want) break;
        __builtin_amdgcn_s_sleep(1);
    }
    asm volatile("" ::: "memory");
    return v;
}
struct cm { f2 w, wr; };
DI cm mkcm(f2 w) { return {w, (f2){-w.y, w.x}}; }
DI f2 cmul(f2 a, cm m) { return __builtin_elementwise_fma((f2){a.y, a.y}, m.wr, (f2){a.x, a.x} * m.w); }
DI f2 cexp_p(float ar2, float air, float p) {
    const float m = __builtin_amdgcn_exp2f(ar2 * p);
    const float t = __builtin_amdgcn_fractf(air * p);
    return (f2){m * __builtin_amdgcn_cosf(t), m * __builtin_amdgcn_sinf(t)};
}
DI h8 as_h8(v4u v) { return __builtin_bit_cast(h8, v); }
DI v4u band_frag(int v0, int width, unsigned one) {
    const unsigned long long m = ((1ull << width) - 1) << 32;
    const unsigned m8 = (unsigned)(m >> (v0 + 32));
    v4u r;
#pragma unroll
    for (int k = 0; k < 4; k++) { const unsigned t = m8 >> (2 * k); r[k] = ((t & 1u) | ((t & 2u) << 15)) * one; }
    return r;
}
DI unsigned opq(unsigned v) { asm volatile("" : "+v"(v)); return v; }
template <typename T> DI T ldsld(unsigned a, int off) { return *(const LAS T*)(size_t)(a + off); }
template <typename T> DI void ldsst(unsigned a, int off, T v) { *(LAS T*)(size_t)(a + off) = v; }
#define MFMA32(a, b, c) __builtin_amdgcn_mfma_f32_32x32x16_f16((a), (b), (c), 0, 0, 0)
#define MFMA16(a, b, c) __builtin_amdgcn_mfma_f32_16x16x32_f16((a), (b), (c), 0, 0, 0)

DI h2 toh2(f2 a) { return (h2){(_Float16)a.x, (_Float16)a.y}; }
DI unsigned cmulh(h2 a, h2 w, h2 wr) { return __builtin_bit_cast(unsigned, __builtin_elementwise_fma((h2){a.y, a.y}, wr, (h2){a.x, a.x} * w)); }
DI void ygen(const float* ar2, const float* air, unsigned rows, int lane) {
    cm w[2]; f2 y[2];
    h2 a[4][2], ar[4][2];
#pragma unroll
    for (int m = 0; m < 2; m++) {
        w[m] = mkcm(cexp_p(ar2[m], air[m], 8.0f)); y[m] = (f2){1.0f, 0.0f};
        const cm a1 = mkcm(cexp_p(ar2[m], air[m], 64.0f));
        const f2 a2 = cmul(a1.w, a1), a3 = cmul(a2, a1);
        a[1][m] = toh2(a1.w); ar[1][m] = toh2(a1.wr); a[2][m] = toh2(a2); ar[2][m] = toh2((f2){-a2.y, a2.x}); a[3][m] = toh2(a3); ar[3][m] = toh2((f2){-a3.y, a3.x});
    }
    const unsigned yb = rows + (lane & 1) * 8;
    const int sl = lane >> 1;
#pragma unroll
    for (int s = 0; s < 8; s++) {
        if ((s & 1) == 0) __builtin_amdgcn_sched_barrier(0);
        const unsigned a0 = yb + ((sl ^ s) << 4), a1 = a0 ^ 128;
        const h2 b0 = toh2(y[0]), b1 = toh2(y[1]);
        ldsst<v2u>(a0, s * 512, (v2u){__builtin_bit_cast(unsigned, b0), __builtin_bit_cast(unsigned, b1)});
#pragma unroll
        for (int c = 1; c < 4; c++) ldsst<v2u>((c & 1) ? a1 : a0, s * 512 + c * 4096, (v2u){cmulh(b0, a[c][0], ar[c][0]), cmulh(b1, a[c][1], ar[c][1])});
        y[0] = cmul(y[0], w[0]); y[1] = cmul(y[1], w[1]);
    }
}

__global__ void __launch_bounds__(1024, 4) k_main(KArgs A) {
    __shared__ __attribute__((aligned(16))) char lds[L_TOT];
    const int b = blockIdx.x, tid = threadIdx.x ^ 512, lane = tid & 63;
    const int wave = __builtin_amdgcn_readfirstlane(tid >> 6);
    const int cg = (b & 7) * 32 + (b >> 3);
    const unsigned ldsb = (unsigned)(size_t)(LAS char*)lds;
    const unsigned pf = __builtin_amdgcn_raw_buffer_load_b32(mkrs((const char*)(__builtin_amdgcn_s_getpc() & ~127ull), CODE_PF), threadIdx.x < 8 ? (((b >> 3) * 8 + threadIdx.x) & 255) * 128 : 0x7ffffff0, 0, 0);
    if (wave == 8) { ((unsigned*)(lds + L_CT))[lane] = 0; asm volatile("s_waitcnt lgkmcnt(0)" ::: "memory"); }
    __builtin_amdgcn_s_barrier();
    __builtin_amdgcn_sched_barrier(0);
    auto rws = mkrs(A.ws, WS_BYTES);
    auto rout = mkrs(A.out, LSEQ * DM * 2);
    const int l31 = lane & 31, hh = lane >> 5, l15 = lane & 15, g4 = lane >> 4;
    const float LOG2E = 1.4426950408889634f, INV2PI = 0.15915494309189535f;
    unsigned pl, pa, pcr, pci, pd;
    if (wave < 4) {
        const int ch = cg * 4 + wave;
        pl = *(const unsigned*)(A.lar + ch * 128 + 2 * lane); pa = *(const unsigned*)(A.aim + ch * 128 + 2 * lane);
        pcr = *(const unsigned*)(A.cre + ch * 128 + 2 * lane); pci = *(const unsigned*)(A.cim + ch * 128 + 2 * lane);
        pd = __builtin_amdgcn_raw_buffer_load_b32(mkrs(A.ldt, DM * 2), (ch & ~1) * 2, 0, 0);
    }
    __builtin_amdgcn_sched_barrier(0);
    int wave2 = wave;
    asm volatile("" : "+s"(wave2));
    if (__builtin_expect(wave2 >= 8, 1)) {
        const int hw = wave - 8, T = tid - 512;
        const unsigned cyc0 = (unsigned)__builtin_amdgcn_s_memtime();
        __builtin_amdgcn_s_setprio(3);
        const int myrow = 16 * (b >> 1) + 2 * (b & 1) + 4 * (hw >> 1) + (hw & 1);
        v4u xv[2], wv[2];
        auto rx = mkrs(A.x, LSEQ * DM * 2);
        auto rw = mkrs(A.wr, DM * 2);
        xv[0] = __builtin_amdgcn_raw_buffer_load_b128(rx, myrow * 2048 + lane * 16, 0, AUX_NT);
        xv[1] = __builtin_amdgcn_raw_buffer_load_b128(rx, myrow * 2048 + 1024 + lane * 16, 0, AUX_NT);
        wv[0] = __builtin_amdgcn_raw_buffer_load_b128(rw, lane * 16, 0, 0);
        wv[1] = __builtin_amdgcn_raw_buffer_load_b128(rw, 1024 + lane * 16, 0, 0);
        unsigned ep = __builtin_amdgcn_raw_buffer_load_b32(rws, WS_EPOCH, 0, AUX_SC1);
        v2u dsk2 = __builtin_amdgcn_raw_buffer_load_b64(mkrs(A.dsk, DM * 2), cg * 8, 0, 0);
        __builtin_amdgcn_sched_barrier(0);
        lds_arrive(lds, C_WL, lane);
        asm volatile("" : "+v"(xv[0]), "+v"(xv[1]));
        *(v4u*)(lds + L_U + hw * LNS + lane * 16) = xv[0];
        *(v4u*)(lds + L_U + hw * LNS + 1024 + lane * 16) = xv[1];
        lds_arrive(lds, C_LN, lane);
        f2 f[8];
#pragma unroll
        for (int e = 0; e < 8; e++) f[e] = (f2){bflo(xv[e >> 2][e & 3]), bfhi(xv[e >> 2][e & 3])};
        f2 s2 = ((f[0] + f[1]) + (f[2] + f[3])) + ((f[4] + f[5]) + (f[6] + f[7]));
        const float mean = wave_sum(s2.x + s2.y) * (1.0f / 1024);
        f2 q2 = {0, 0};
#pragma unroll
        for (int e = 0; e < 8; e++) { const f2 d = f[e] - mean; q2 = __builtin_elementwise_fma(d, d, q2); }
        asm volatile("" : "+v"(wv[0]), "+v"(wv[1]));
        float xw = 0, sw = 0;
#pragma unroll
        for (int e = 0; e < 8; e++) { xw = dot2(xv[e >> 2][e & 3], wv[e >> 2][e & 3], xw); sw = dot2(0x3f803f80u, wv[e >> 2][e & 3], sw); }
        const float mbf = rbf(mean);
        const float var = wave_sum(q2.x + q2.y) * (1.0f / 1024);
        const float rn = wave_sum(fmaf(-mbf, sw, xw));
        const float sd = rbf(__builtin_amdgcn_sqrtf(rbf(rbf(var) + 1e-5f)));
        const unsigned ms = (__float_as_uint(mbf) >> 16) | (__float_as_uint(sd) & 0xffff0000u);
        const float r = rn * __builtin_amdgcn_rcpf(sd);
        const unsigned rb = pkbf(r, r) & 0xffffu;
        const unsigned key = (rb & 0x8000u) ? (~rb & 0xffffu) : (rb | 0x8000u);
        asm volatile("s_mov_b64 exec, 1\n\tds_write_b32 %0, %1\n\tds_max_u32 %2, %3\n\ts_mov_b64 exec, -1"
                     :: "v"(ldsb + L_CT + (C_ST + hw) * 4), "v"(ms), "v"(ldsb + L_CT + C_CMAX * 4), "v"((key << 16) | (unsigned)(2047 - myrow)) : "memory");
        asm volatile("" : "+v"(ep), "+v"(dsk2));
        const unsigned nar = lds_arrive_rtn(lds, C_CN, lane);
        const unsigned tag = ep + 1u;
        if (nar == 7) {
            const unsigned k2 = *ctl(lds, C_CMAX);
            const unsigned br = 2047 - (k2 & 0xffffu);
            const unsigned mb = *ctl(lds, C_ST + ((br >> 1) & 6) + (br & 1));
            if (lane == 0) __builtin_amdgcn_raw_buffer_store_b128((v4u){k2, tag, mb, tag}, rws, WS_CAND + b * 16, 0, AUX_SC1);
        }
        lds_wait_ge(lds, C_LN, 8);
        {
            const char* const sp = lds + L_U + (lane & 3) * 2 * LNS + (T >> 2) * 8;
            const v2u a0 = *(const v2u*)sp, a1 = *(const v2u*)(sp + LNS), b0 = *(const v2u*)(sp + 1024), b1 = *(const v2u*)(sp + LNS + 1024);
            const unsigned o = WS_XT + ((T >> 3) * 256 + ((b + (T >> 3)) & 255)) * 128 + (lane & 7) * 16;
            __builtin_amdgcn_raw_buffer_store_b128((v4u){a0.x, a0.y, a1.x, a1.y}, rws, o, 0, AUX_SC1);
            __builtin_amdgcn_raw_buffer_store_b128((v4u){b0.x, b0.y, b1.x, b1.y}, rws, WS_XT + (((T >> 3) + 64) * 256 + ((b + (T >> 3) + 64) & 255)) * 128 + (lane & 7) * 16, 0, AUX_SC1);
        }
        *ctl(lds, C_TAG) = tag;
        lds_arrive(lds, C_SI, lane);
        asm volatile("s_waitcnt vmcnt(0)" :: "v"(pf) : "memory");
        if (lds_arrive_rtn(lds, C_DR, lane) == 7) {
            const v2u m2 = *(const v2u*)ctl(lds, C_ST + 2 * (lane & 3));
            if (lane < 8) __builtin_amdgcn_raw_buffer_store_b128((v4u){m2.x, tag, m2.y, tag}, rws, WS_REC + b * 128 + lane * 16, 0, AUX_SC1);
        }
        __builtin_amdgcn_s_setprio(1);
        const int g4s = ((g4 & 1) << 1) | (g4 >> 1);
        if (hw < 7) {
            const int v = 8 * ((hw == 3 || hw == 5) ? g4s : g4) + l15;
            const int o = (hw == 0 ? 18 : hw == 1 ? 21 : hw == 2 ? 26 : hw == 3 ? 21 : hw == 4 ? 20 : hw == 5 ? 19 : 18);
            const int w = hw == 0 ? 3 : hw == 1 ? 5 : hw == 2 ? 3 : hw == 3 ? 5 : hw == 4 ? 7 : hw == 5 ? 9 : 2;
            *(v4u*)(lds + L_BD + hw * 1024 + lane * 16) = band_frag(v - o, w, hw == 6 ? 0xffffu : 0x3C00u);
        }
        lds_arrive(lds, C_BD, lane);
        v4u cn[4];
        auto cand_issue = [&]() {
#pragma unroll
            for (int j = 0; j < 4; j++) cn[j] = __builtin_amdgcn_raw_buffer_load_b128(rws, WS_CAND + (lane + 64 * j) * 16, 0, AUX_SC1);
        };
        auto cand_finish = [&]() {
            _Pragma("nounroll") for (unsigned s = 0; s < SPIN_CAP; s++) {
                bool ok = true;
#pragma unroll
                for (int j = 0; j < 4; j++) if (((cn[j].y ^ tag) | (cn[j].w ^ tag)) != 0) { ok = false; cn[j] = __builtin_amdgcn_raw_buffer_load_b128(rws, WS_CAND + (lane + 64 * j) * 16, 0, AUX_SC1); }
                if (__all(ok)) break;
                __builtin_amdgcn_s_sleep(6);
                asm volatile("" ::: "memory");
            }
            const unsigned mk = max(max(cn[0].x, cn[1].x), max(cn[2].x, cn[3].x));
            const unsigned bst = wave_umax(mk);
            if (mk == bst) {
                *ctl(lds, C_MS) = cn[0].x == bst ? cn[0].z : cn[1].x == bst ? cn[1].z : cn[2].x == bst ? cn[2].z : cn[3].z;
                asm volatile("" ::: "memory");
                __hip_atomic_store(ctl(lds, C_IDX), bst | 0x8000u, __ATOMIC_RELAXED, __HIP_MEMORY_SCOPE_WORKGROUP);
                if (b == 0) __builtin_amdgcn_raw_buffer_store_b32(tag, rws, WS_EPOCH, 0, AUX_SC1);
            }
        };
        if (hw == 0) {
            while ((unsigned)__builtin_amdgcn_s_memtime() - cyc0 < CAND_T) __builtin_amdgcn_s_sleep(1);
            __builtin_amdgcn_s_setprio(3);
            cand_issue();
            cand_finish();
            __builtin_amdgcn_s_setprio(1);
        }
        while ((unsigned)__builtin_amdgcn_s_memtime() - cyc0 < FL_T) __builtin_amdgcn_s_sleep(1);
        v4u gs[2], gx[2];
        {
            const unsigned ro = WS_REC + (T >> 2) * 256 + (lane & 3) * 16;
            gs[0] = __builtin_amdgcn_raw_buffer_load_b128(rws, ro, 0, AUX_SC1);
            gs[1] = __builtin_amdgcn_raw_buffer_load_b128(rws, ro + 128, 0, AUX_SC1);
            _Pragma("nounroll") for (unsigned s = 0; s < SPIN_CAP; s++) {
                bool ok = true;
#pragma unroll
                for (int j = 0; j < 2; j++) if (((gs[j].y ^ tag) | (gs[j].w ^ tag)) != 0) { ok = false; gs[j] = __builtin_amdgcn_raw_buffer_load_b128(rws, ro + 128 * j, 0, AUX_SC1); }
                if (__all(ok)) break;
                __builtin_amdgcn_s_sleep(6);
                asm volatile("" ::: "memory");
            }
            asm volatile("" ::: "memory");
            __builtin_amdgcn_s_setprio(3);
            const int cp = cg >> 1, p0 = 2 * (T >> 2);
            const unsigned xo = WS_XT + cp * 32768 + (cg & 1) * 64 + (lane & 3) * 16;
            gx[0] = __builtin_amdgcn_raw_buffer_load_b128(rws, xo + ((p0 + cp) & 255) * 128, 0, AUX_SC1);
            gx[1] = __builtin_amdgcn_raw_buffer_load_b128(rws, xo + ((p0 + 1 + cp) & 255) * 128, 0, AUX_SC1);
            __builtin_amdgcn_sched_barrier(0);
        }
        asm volatile("" : "+v"(gx[0]), "+v"(gx[1]));
        float base[4][4];
        {
            const float d1[4] = {(1.0f + bflo(dsk2.x)) * (1.0f / 64), (1.0f + bfhi(dsk2.x)) * (1.0f / 64), (1.0f + bflo(dsk2.y)) * (1.0f / 64), (1.0f + bfhi(dsk2.y)) * (1.0f / 64)};
            float un[4][4];
#pragma unroll
            for (int r = 0; r < 4; r++) {
                const unsigned msr = gs[r >> 1][2 * (r & 1)];
                const float rinv = __builtin_amdgcn_rcpf(bfhi(msr)) * 64.0f, mr = -bflo(msr) * rinv;
#pragma unroll
                for (int p = 0; p < 2; p++) {
                    const unsigned xw = gx[r >> 1][(r & 1) * 2 + p];
                    const f2 xx = {bflo(xw), bfhi(xw)};
                    const f2 u = __builtin_elementwise_fma(xx, (f2){rinv, rinv}, (f2){mr, mr});
                    const f2 bs = __builtin_elementwise_fma(u, (f2){d1[2 * p], d1[2 * p + 1]}, xx);
                    un[r][2 * p] = u.x; un[r][2 * p + 1] = u.y; base[r][2 * p] = bs.x; base[r][2 * p + 1] = bs.y;
                }
            }
            const int off = uslot((T >> 1) + 68) * 16 + ((T & 1) ? 0 : 8);
            lds_wait_ge(lds, C_SI, 8);
#pragma unroll
            for (int c = 0; c < 4; c++) *(v2u*)(lds + L_U + c * UB + off) = (v2u){pkh(un[3][c], un[2][c]), pkh(un[1][c], un[0][c])};
            if (hw < 4 && l31 < 17) {
                *(v4u*)(lds + L_U + hw * UB + (96 * hh + l31) * 16) = (v4u){0, 0, 0, 0};
                *(v4u*)(lds + L_U + hw * UB + (200 + 96 * hh + l31) * 16) = (v4u){0, 0, 0, 0};
            }
        }
        lds_arrive(lds, hw < 4 ? C_U : C_U1, lane);
        if (hw >= 4) {
            const int c = hw - 4;
            char* const U = lds + L_U + c * UB;
            char* const R = lds + L_XY + c * XYB;
            lds_wait_ge(lds, C_T + c, 1);
            const int e4 = 79 - l15 + 4 * g4, e8 = 127 - l15 + 4 * g4;
            const char* const f4b = R + O_FT + (e4 & 1) * 192 + (e4 >> 1) * 4;
            const char* const f8b = R + O_FT + (e8 & 1) * 192 + (e8 >> 1) * 4;
            auto ldTf = [&](const char* bp, int ka, int d) {
                const unsigned* pa_ = (const unsigned*)(bp + ka * 384 - 32 * d); const unsigned* pb_ = (const unsigned*)(bp + (ka + 1) * 384 - 32 * d);
                return (v4u){pa_[0], pa_[1], pb_[0], pb_[1]};
            };
            lds_wait_ge(lds, C_BD, 8);
            v4u bd[4];
#pragma unroll
            for (int k = 0; k < 4; k++) bd[k] = *(const v4u*)(lds + L_BD + k * 1024 + lane * 16);
            v4u t01[3], t23[3];
#pragma unroll
            for (int d = 0; d < 3; d++) { t01[d] = ldTf(f4b, 0, d + 2); t23[d] = ldTf(f4b, 2, d + 2); }
            lds_wait_ge(lds, C_U, 4);
            const char* const uap = U + uslot(2 * l15 + 1 - g4 + 68) * 16;
            const char* const ubp = U + uslot(2 * l15 - g4s + 68) * 16;
            const f4 z4 = {0, 0, 0, 0};
            auto pkW = [&](f4 a, f4 b_) { return (v4u){pkh(a[0], a[1]), pkh(a[2], a[3]), pkh(b_[0], b_[1]), pkh(b_[2], b_[3])}; };
            f4 F[8];
#pragma unroll
            for (int t = 2; t < 8; t++) F[t] = z4;
            const int s0 = 4 * l15 + g4;
            char* const fw = R + O_YF + (s0 + (s0 >> 3)) * 16;
#pragma unroll
            for (int tp = 0; tp < 6; tp++) {
                const v4u ua = *(const v4u*)(uap + tp * 128), ub = *(const v4u*)(ubp + tp * 128);
                const v4u w01 = pkW(MFMA16(as_h8(ua), as_h8(bd[0]), z4), MFMA16(as_h8(ua), as_h8(bd[1]), z4));
                const v4u w23 = pkW(MFMA16(as_h8(ua), as_h8(bd[2]), z4), MFMA16(as_h8(ub), as_h8(bd[3]), z4));
#pragma unroll
                for (int d = 2; d <= 4; d++) if (tp + d < 8) {
                    F[tp + d] = MFMA16(as_h8(w01), as_h8(t01[d - 2]), F[tp + d]);
                    F[tp + d] = MFMA16(as_h8(w23), as_h8(t23[d - 2]), F[tp + d]);
                }
                if (tp == 1) {
                    *(f4*)(fw + 72 * 16 * 2) = F[2]; *(f4*)(fw + 72 * 16 * 3) = F[3];
                    lds_arrive(lds, C_Y0, lane);
                    __builtin_amdgcn_s_setprio(1);
                    lds_wait_ge(lds, C_U1, 4);
                }
            }
            __builtin_amdgcn_sched_barrier(0);
            {
                v4u t45[4];
#pragma unroll
                for (int d = 0; d < 4; d++) t45[d] = ldTf(f8b, 4, d + 4);
                const v4u b4 = *(const v4u*)(lds + L_BD + 4096 + lane * 16), b5 = *(const v4u*)(lds + L_BD + 5120 + lane * 16);
                const v4u an = *(const v4u*)(lds + L_BD + 6144 + lane * 16) & *ctl(lds, C_K1024 + c);
#pragma unroll
                for (int tp = 0; tp < 4; tp++) {
                    const v4u ua = *(const v4u*)(uap + tp * 128), ub = *(const v4u*)(ubp + tp * 128);
                    const v4u w45 = pkW(MFMA16(as_h8(ua), as_h8(b4), z4), MFMA16(as_h8(ub), as_h8(b5), z4));
                    F[tp + 4] = MFMA16(as_h8(an), as_h8(ua), F[tp + 4]);
#pragma unroll
                    for (int d = 4; d <= 7; d++) if (tp + d < 8) F[tp + d] = MFMA16(as_h8(w45), as_h8(t45[d - 4]), F[tp + d]);
                }
            }
#pragma unroll
            for (int t = 4; t < 8; t++) *(f4*)(fw + 72 * 16 * t) = F[t];
            lds_arrive(lds, C_Y1, lane);
        }
        const unsigned idx = 2047 - (lds_wait_ge(lds, C_IDX, 1) & 2047u);
        __builtin_amdgcn_s_setprio(3);
        lds_wait_ge(lds, hw >= 4 ? C_Y1 : C_Y0, 8);
        const f4 osc = *(const f4*)ctl(lds, C_OSC);
        __builtin_amdgcn_s_setprio(0);
        {
            const char* const yp = lds + L_XY + (T + (T >> 3)) * 16;
            f4 yv[4];
#pragma unroll
            for (int c = 0; c < 4; c++) yv[c] = *(const f4*)(yp + c * XYB + O_YD);
            if (hw >= 2) {
#pragma unroll
                for (int c = 0; c < 4; c++) yv[c] += *(const f4*)(yp + c * XYB + O_YF);
            }
#pragma unroll
            for (int r = 0; r < 4; r++) {
                const int row = 4 * T + r;
                const float o0 = fmaf(yv[0][r], osc[0], base[r][0]), o1 = fmaf(yv[1][r], osc[1], base[r][1]), o2 = fmaf(yv[2][r], osc[2], base[r][2]), o3 = fmaf(yv[3][r], osc[3], base[r][3]);
                __builtin_amdgcn_raw_buffer_store_b64((v2u){pkbf(o0, o1), pkbf(o2, o3)}, rout, row == (int)idx ? 0x7ffffff0 : row * 2048 + cg * 8, 0, 0);
                if (row == (int)idx) {
                    *(f4*)ctl(lds, C_ROWV) = (f4){o0, o1, o2, o3};
                    asm volatile("s_waitcnt lgkmcnt(0)" ::: "memory");
                    __hip_atomic_store(ctl(lds, C_ROWF), 1u, __ATOMIC_RELAXED, __HIP_MEMORY_SCOPE_WORKGROUP);
                }
            }
        }
        __builtin_amdgcn_endpgm();
    } else if (wave2 < 4) {
        __builtin_amdgcn_s_setprio(2);
        asm volatile("" : "+v"(pl), "+v"(pa), "+v"(pcr), "+v"(pci), "+v"(pd));
        const float ldt = (wave & 1) ? bfhi(pd) : bflo(pd);
        char* const R = lds + L_XY + wave * XYB;
        {
            const float dt = rbf(fexp(ldt));
            float ar2[2], air[2];
            ar2[0] = (-rbf(fexp(bflo(pl))) * dt) * LOG2E; ar2[1] = (-rbf(fexp(bfhi(pl))) * dt) * LOG2E;
            air[0] = (bflo(pa) * dt) * INV2PI; air[1] = (bfhi(pa) * dt) * INV2PI;
            float mx = fmaxf(fabsf(bflo(pcr)) + fabsf(bflo(pci)), fabsf(bfhi(pcr)) + fabsf(bfhi(pci))) * (2.0f * dt);
            mx = wave_max(mx);
            int E = (__float_as_uint(mx) >> 23) & 0xff; E = min(max(E, 20), 240);
            const float cs = __uint_as_float((unsigned)(262 - E) << 23);
            if (lane == 0) *ctl(lds, C_OSC + wave) = (unsigned)(E - 13) << 23;
            f2 C[2] = {(f2){bflo(pcr) * dt * cs, -(bflo(pci) * dt * cs)}, (f2){bfhi(pcr) * dt * cs, -(bfhi(pci) * dt * cs)}};
            const unsigned xb = ldsb + L_XY + wave * XYB + (lane & 1) * 8;
            const int sl = lane >> 1;
            auto putX = [&](int row, const f2* a) { ldsst<v2u>(xb + ((sl ^ (row & 15)) << 4), row * 512, (v2u){pkh(a[0].x, a[0].y), pkh(a[1].x, a[1].y)}); };
            auto mul2 = [&](f2* o, const f2* a, const cm* m) { o[0] = cmul(a[0], m[0]); o[1] = cmul(a[1], m[1]); };
            cm z[2], zb[2], z2[2]; f2 t[2], p256[2], p258[2], p260[2], p262[2], p512[2], p516[2], p772[2];
#pragma unroll
            for (int m = 0; m < 2; m++) { z[m] = mkcm(cexp_p(ar2[m], -air[m], 1.0f)); zb[m] = mkcm(cexp_p(ar2[m], -air[m], 256.0f)); z2[m] = mkcm(cmul(z[m].w, z[m])); t[m] = C[m]; }
#pragma unroll
            for (int e = 0; e < 8; e++) { putX(e, t); if (e < 7) mul2(t, t, z); }
            mul2(p256, C, zb); mul2(p258, p256, z2); mul2(p260, p258, z2); mul2(p262, p260, z2);
            putX(8, p256); putX(9, p258); putX(10, p260); putX(11, p262);
            mul2(p512, p256, zb); mul2(p516, p260, zb); mul2(t, p512, zb); mul2(p772, p516, zb);
            putX(12, p512); putX(13, p516); putX(14, t); putX(15, p772);
#pragma unroll
            for (int e = 0; e < 5; e++) { mul2(t, t, zb); putX(16 + e, t); }
            ygen(ar2, air, ldsb + L_XY + wave * XYB + 21 * 512, lane);
        }
        const int o1 = 64 + 16 * l31 + 8 * hh;
        int ad[4];
        {
            const int bo = l31 & 1;
            int a0, a1, st2;
            if (hh) { a0 = (bo ? 1 : 3) * 384 + 2 * (79 - (31 + ((l31 + 1) >> 1))); a1 = (bo ? 2 : 0) * 384 + 2 * (79 - (32 + (l31 >> 1))); st2 = 32; }
            else { a0 = (bo ? 4 : 5) * 384 + 2 * (127 - (63 + ((l31 + 1) >> 1))); a1 = a0 - 32; st2 = 64; }
            ad[0] = a0; ad[1] = a1; ad[2] = a0 - st2; ad[3] = a1 - st2;
            if (lane == 0) ad[0] = 3 * 384 + 2 * 16;
            if (lane == 32) ad[0] = 3 * 384 + 2 * 70;
#pragma unroll
            for (int e = 0; e < 4; e++) ad[e] = opq(ad[e] + ldsb + L_XY + wave * XYB + O_FT);
        }
        f16v kacc;
#pragma unroll
        for (int e = 0; e < 16; e++) kacc[e] = 0.0f;
        {
            const int lp = hh ^ (l31 & 15);
            v4u xa[16], yb[16];
#pragma unroll
            for (int t = 0; t < 16; t++) {
                const int so = ((lp ^ ((2 * t) & 15)) + ((2 * t) & 16)) << 4;
                xa[t] = *(const v4u*)(R + l31 * 512 + so);
                yb[t] = *(const v4u*)(R + (21 + l31) * 512 + so);
                if (t >= 4) kacc = MFMA32(as_h8(xa[t - 4]), as_h8(yb[t - 4]), kacc);
                __builtin_amdgcn_sched_barrier(0);
            }
            const v4u z = {0, 0, 0, 0};
            *(v4u*)(R + O_FT + lane * 16) = z; *(v4u*)(R + O_FT + 1024 + lane * 16) = z; if (lane < 16) *(v4u*)(R + O_FT + 2048 + lane * 16) = z;
            { const int cp = lane >> 4, o = lane & 15; *(v4u*)(R + O_KD + cp * 1344 + (o < 5 ? o * 16 : 1072 + (o - 5) * 16)) = z; }
#pragma unroll
            for (int t = 12; t < 16; t++) kacc = MFMA32(as_h8(xa[t]), as_h8(yb[t]), kacc);
        }
        {
            char* const KD = R + O_KD;
            char* const FT = R + O_FT;
            float kv[12];
#pragma unroll
            for (int e = 0; e < 12; e++) kv[e] = kacc[e];
            const f2 sw = swap32(kv[0], kv[0]);
            float n0 = hh ? dppf<0x130>(sw.x) : sw.y;
            const float k256 = __uint_as_float(__builtin_amdgcn_readlane(__float_as_uint(kv[4]), 0));
            n0 = lane == 63 ? k256 : n0;
            const unsigned p01 = pkh(kv[0], kv[1]), p23 = pkh(kv[2], kv[3]), p12 = pkh(kv[1], kv[2]), p3n = pkh(kv[3], n0);
            *(v2u*)(KD + o1) = (v2u){p01, p23};
            *(v2u*)(KD + KD1 + o1) = (v2u){p12, p3n};
            *(unsigned*)(KD + KD2 + o1 - 4) = p01; *(unsigned*)(KD + KD2 + o1) = p23;
            *(unsigned*)(KD + KD3 + o1 - 4) = p12; *(unsigned*)(KD + KD3 + o1) = p3n;
            const float vn = dppf<0x130>(kv[4]);
            const unsigned q00 = pkh(kv[4], kv[4]), q22 = pkh(kv[6], kv[6]);
            if (hh == 0) {
                const v4u ev = {q00, pkh(kv[5], kv[6]), q22, pkh(kv[7], vn)};
                const v4u od = {pkh(kv[4], kv[5]), q22, pkh(kv[6], kv[7]), pkh(vn, vn)};
                const int o2 = 576 + 16 * l31;
                *(v4u*)(KD + o2) = ev;
                *(v2u*)(KD + KD1 + o2) = (v2u){od[0], od[1]}; *(v2u*)(KD + KD1 + o2 + 8) = (v2u){od[2], od[3]};
                unsigned* const e2 = (unsigned*)(KD + KD2 + o2 - 4); unsigned* const o3 = (unsigned*)(KD + KD3 + o2 - 4);
#pragma unroll
                for (int k = 0; k < 4; k++) { e2[k] = ev[k]; o3[k] = od[k]; }
            }
            if (lane == 0) { *(u16*)(KD + KD1 + 62) = (u16)p01; *(u16*)(KD + KD3 + 58) = (u16)p01; *ctl(lds, C_K1024 + wave) = pkh(kv[8], kv[8]); }
            if (lane == 32) {
                const unsigned q0z = q00 & 0xffffu;
                *(v2u*)(KD + 1088) = (v2u){q00, q0z};
                *(unsigned*)(KD + KD2 + 1084) = q00; *(unsigned*)(KD + KD2 + 1088) = q0z;
                *(unsigned*)(KD + KD1 + 1088) = q00; *(unsigned*)(KD + KD3 + 1084) = q00;
                *(u16*)(FT + 5 * 384) = (u16)pkh(kv[8], kv[8]);
            }
#pragma unroll
            for (int e = 0; e < 4; e++) {
                const u16 hv = (u16)pkh(hh ? kv[4 + e] : kv[8 + e], 0.0f);
                ldsst<u16>(ad[e], 0, hv); ldsst<u16>(ad[e], 190, hv);
            }
        }
        lds_arrive(lds, C_T + wave, lane);
        const int rR = 16 * ((l31 >> 2) & 1) + 4 * (l31 >> 3) + (l31 & 3);
        const int tau = rR + 8 * hh + 1;
        const unsigned ka0 = opq(ldsb + L_XY + wave * XYB + O_KD + (tau & 3) * 1344 + ((tau & 3) ? 8 : 0) + (tau >> 2) * 8);
        const unsigned ka1 = opq(ka0 + 8);
        unsigned ub = ldsb + L_U + wave * UB + (l31 + 96 - 96 * hh) * 16;
        unsigned yw = ldsb + L_XY + wave * XYB + O_YD + (9 * l31 + 4 * hh) * 16;
        constexpr int PD = 4;
        v2u kf0[35], kf1[35];
#pragma unroll
        for (int j = 0; j < PD; j++) { kf0[j] = ldsld<v2u>(ka0, 32 * j); kf1[j] = ldsld<v2u>(ka1, 32 * j); }
        lds_wait_ge(lds, C_U, 4);
        __builtin_amdgcn_s_setprio(2);
#pragma unroll 1
        for (int be = 0; be < 2; be++) {
            auto uoff = [](int j) { const int t = 3 - 2 * j; const int fl = (t >= 0) ? (t >> 2) : -((-t + 3) >> 2); return (uplane(t & 3) - 96 + 17 + fl) * 16; };
            v4u uf[35], pk, pu;
#pragma unroll
            for (int j = 0; j < PD; j++) uf[j] = ldsld<v4u>(ub, uoff(j));
            f16v acc;
#pragma unroll
            for (int e = 0; e < 16; e++) acc[e] = 0.0f;
            __builtin_amdgcn_sched_barrier(0);
#pragma unroll
            for (int j = 0; j < 35; j++) {
                const v4u kj = {kf0[j].x, kf0[j].y, kf1[j].x, kf1[j].y};
                const int jn = (j + PD) % 35;
                v2u n0 = ldsld<v2u>(ka0, 32 * jn), n1 = ldsld<v2u>(ka1, 32 * jn);
                if (j + PD < 35) uf[j + PD] = ldsld<v4u>(ub, uoff(j + PD));
                if (j > 0) asm volatile("" :: "v"(pk), "v"(pu));
                __builtin_amdgcn_sched_barrier(0);
                acc = MFMA32(as_h8(kj), as_h8(uf[j]), acc);
                pk = kj; pu = uf[j];
                kf0[jn] = n0; kf1[jn] = n1;
                __builtin_amdgcn_sched_barrier(0);
            }
#pragma unroll
            for (int g = 0; g < 4; g++) ldsst<f4>(yw, g * 16, (f4){acc[4 * g], acc[4 * g + 1], acc[4 * g + 2], acc[4 * g + 3]});
            lds_arrive(lds, C_Y0 + be, lane);
            __builtin_amdgcn_s_setprio(1);
            ub += 32 * 16; yw += 288 * 16;
            lds_wait_ge(lds, C_U1, 4);
        }
        __builtin_amdgcn_endpgm();
    } else {
        const int wv = wave - 4;
        const bool moe = (b >> 3) < 28;
        const int mo = (b & 7) * 28 + (b >> 3), fb = mo >> 2, kq = mo & 3;
        const int k0 = kq * 256 + wv * 64 + (lane >> 3) * 8;
        v4u wgv[8], wuv[8];
        auto rg = mkrs(A.wg, moe ? DM * FF * 2 : 0), ru = mkrs(A.wu, moe ? DM * FF * 2 : 0);
        auto ldw = [&](int i) {
            wgv[i] = __builtin_amdgcn_raw_buffer_load_b128(rg, (k0 + i) * 7168 + fb * 128 + (lane & 7) * 16, 0, AUX_NT);
            wuv[i] = __builtin_amdgcn_raw_buffer_load_b128(ru, (k0 + i) * 7168 + fb * 128 + (lane & 7) * 16, 0, AUX_NT);
        };
        if (!moe && wv != 0) __builtin_amdgcn_endpgm();
        lds_wait_ge(lds, C_WL, 8);
#pragma unroll
        for (int i = 0; i < 8; i++) {
            if (i == 2) lds_wait_ge(lds, C_DR, 8);
            if (i >= 5) asm volatile("s_waitcnt vmcnt(4)" ::: "memory");
            ldw(i);
            __builtin_amdgcn_sched_barrier(0);
        }
        v2u wdv[16];
        auto rd = mkrs(A.wd, moe ? DM * FF * 2 : 0);
        lds_wait_ge(lds, C_SI, 1);
        const unsigned tag = *ctl(lds, C_TAG);
        const unsigned best = lds_wait_ge(lds, C_IDX, 1);
        __builtin_amdgcn_s_setprio(3);
        const unsigned tidx = (unsigned)__builtin_amdgcn_s_memtime();
        const unsigned idx = 2047 - (best & 2047u);
        const unsigned key = best >> 16;
        const float rsel = bflo((key & 0x8000u) ? (key & 0x7fffu) : (~key & 0xffffu));
        bool fin = true;
        if (moe) {
            const v4u xs = __builtin_amdgcn_raw_buffer_load_b128(mkrs(A.x, LSEQ * DM * 2), idx * 2048 + k0 * 2, 0, 0);
            __builtin_amdgcn_sched_barrier(0);
#pragma unroll
            for (int i = 0; i < 8; i += 2)
#pragma unroll
                for (int c = 0; c < 4; c++) {
                    const unsigned g0 = wgv[i][c], g1 = wgv[i + 1][c], u0 = wuv[i][c], u1 = wuv[i + 1][c];
                    wgv[i][c] = __builtin_amdgcn_perm(g1, g0, 0x05040100u); wgv[i + 1][c] = __builtin_amdgcn_perm(g1, g0, 0x07060302u);
                    wuv[i][c] = __builtin_amdgcn_perm(u1, u0, 0x05040100u); wuv[i + 1][c] = __builtin_amdgcn_perm(u1, u0, 0x07060302u);
                }
#pragma unroll
            for (int i = 0; i < 8; i++) asm volatile("" : "+v"(wgv[i]), "+v"(wuv[i]));
            const unsigned ms = *ctl(lds, C_MS);
            const float m = bflo(ms), rinv = __builtin_amdgcn_rcpf(bfhi(ms));
            float acc[16];
#pragma unroll
            for (int c = 0; c < 16; c++) acc[c] = 0;
#pragma unroll
            for (int i2 = 0; i2 < 4; i2++) {
                const unsigned dp = pkbf(bflo(xs[i2]) - m, bfhi(xs[i2]) - m);
                const unsigned np = pkbf(bflo(dp) * rinv, bfhi(dp) * rinv);
#pragma unroll
                for (int c = 0; c < 4; c++) {
                    acc[2 * c] = dot2(wgv[2 * i2][c], np, acc[2 * c]); acc[2 * c + 1] = dot2(wgv[2 * i2 + 1][c], np, acc[2 * c + 1]);
                    acc[8 + 2 * c] = dot2(wuv[2 * i2][c], np, acc[8 + 2 * c]); acc[8 + 2 * c + 1] = dot2(wuv[2 * i2 + 1][c], np, acc[8 + 2 * c + 1]);
                }
            }
            float r8[8], r4[4], r2[2];
#pragma unroll
            for (int c = 0; c < 8; c++) { f2 t = swap32(acc[c], acc[8 + c]); r8[c] = t.x + t.y; }
#pragma unroll
            for (int c = 0; c < 4; c++) { f2 t = swap16(r8[c], r8[4 + c]); r4[c] = t.x + t.y; }
            const bool b3 = lane & 8;
#pragma unroll
            for (int c = 0; c < 2; c++) { const float keep = b3 ? r4[2 + c] : r4[c], give = b3 ? r4[c] : r4[2 + c]; r2[c] = keep + dppf<0x128>(give); }
            const int vi = (lane >> 5) * 64 + 8 * (lane & 7) + 4 * ((lane >> 4) & 1) + 2 * ((lane >> 3) & 1);
            *(f2*)(lds + L_GUR + wv * 512 + vi * 4) = (f2){r2[0], r2[1]};
            if (lds_arrive_rtn(lds, C_GU, lane) == 3) {
                const f2 s0 = *(const f2*)(lds + L_GUR + lane * 8), s1 = *(const f2*)(lds + L_GUR + 512 + lane * 8), s2 = *(const f2*)(lds + L_GUR + 1024 + lane * 8), s3 = *(const f2*)(lds + L_GUR + 1536 + lane * 8);
                const f2 s = (s0 + s1) + (s2 + s3);
                __builtin_amdgcn_raw_buffer_store_b128((v4u){__float_as_uint(s.x), tag, __float_as_uint(s.y), tag}, rws, WS_GU + mo * 1024 + lane * 16, 0, AUX_SC1);
            }
            __builtin_amdgcn_sched_barrier(0);
#pragma unroll
            for (int i = 0; i < 16; i++) wdv[i] = __builtin_amdgcn_raw_buffer_load_b64(rd, (fb * 64 + wv * 16 + i) * 2048 + kq * 512 + lane * 8, 0, AUX_NT);
            __builtin_amdgcn_sched_barrier(0);
            __builtin_amdgcn_s_sleep(16);
#pragma unroll
            for (int i = 0; i < 16; i += 2) {
                const v2u a = wdv[i], c = wdv[i + 1];
                wdv[i] = (v2u){__builtin_amdgcn_perm(c.x, a.x, 0x05040100u), __builtin_amdgcn_perm(c.x, a.x, 0x07060302u)};
                wdv[i + 1] = (v2u){__builtin_amdgcn_perm(c.y, a.y, 0x05040100u), __builtin_amdgcn_perm(c.y, a.y, 0x07060302u)};
            }
#pragma unroll
            for (int i = 0; i < 16; i++) asm volatile("" : "+v"(wdv[i]));
            v2u gq, uq;
            _Pragma("nounroll") for (unsigned s = 0; s < SPIN_CAP; s++) {
                gq = __builtin_amdgcn_raw_buffer_load_b64(rws, WS_GU + (fb * 4 + g4) * 1024 + (16 * wv + l15) * 8, 0, AUX_SC1);
                uq = __builtin_amdgcn_raw_buffer_load_b64(rws, WS_GU + (fb * 4 + g4) * 1024 + 512 + (16 * wv + l15) * 8, 0, AUX_SC1);
                if (__all(((gq.y ^ tag) | (uq.y ^ tag)) == 0)) break;
                __builtin_amdgcn_s_sleep(4);
                asm volatile("" ::: "memory");
            }
            const float gt = rbf(sum32(sum16(__uint_as_float(gq.x)))), ut = rbf(sum32(sum16(__uint_as_float(uq.x))));
            const float hv = rbf(gt * __builtin_amdgcn_rcpf(1.0f + fexp(-gt))) * ut;
            const unsigned hp = pkbf(hv, dppf<0x130>(hv));
            float p4[4] = {0, 0, 0, 0};
#pragma unroll
            for (int i = 0; i < 16; i += 2) {
                const unsigned hs = __builtin_amdgcn_readlane(hp, i);
                p4[0] = dot2(wdv[i].x, hs, p4[0]); p4[1] = dot2(wdv[i].y, hs, p4[1]);
                p4[2] = dot2(wdv[i + 1].x, hs, p4[2]); p4[3] = dot2(wdv[i + 1].y, hs, p4[3]);
            }
            *(f4*)(lds + L_PR + wv * 1024 + lane * 16) = (f4){p4[0], p4[1], p4[2], p4[3]};
            fin = false;
            if (lds_arrive_rtn(lds, C_P, lane) == 3) {
#pragma unroll
                for (int k = 0; k < 2; k++) {
                    const char* const pp = lds + L_PR + k * 512 + lane * 8;
                    const f2 s = (*(const f2*)pp + *(const f2*)(pp + 1024)) + (*(const f2*)(pp + 2048) + *(const f2*)(pp + 3072));
                    __builtin_amdgcn_raw_buffer_store_b128((v4u){__float_as_uint(s.x), tag, __float_as_uint(s.y), tag}, rws, WS_P + fb * 8192 + kq * 2048 + k * 1024 + lane * 16, 0, AUX_SC1);
                }
                fin = true;
            }
        }
        if (fin) {
            while ((unsigned)__builtin_amdgcn_s_memtime() - tidx < H3_T) __builtin_amdgcn_s_sleep(1);
            __builtin_amdgcn_s_sleep(24);
            v4u p0, p1;
            const bool on = lane < 48;
            _Pragma("nounroll") for (unsigned s = 0; s < SPIN_CAP; s++) {
                p0 = __builtin_amdgcn_raw_buffer_load_b128(rws, WS_P + (lane >> 1) * 8192 + cg * 32 + (lane & 1) * 16, 0, AUX_SC1);
                p1 = __builtin_amdgcn_raw_buffer_load_b128(rws, WS_P + ((on ? 32 : 0) + (lane >> 1)) * 8192 + cg * 32 + (lane & 1) * 16, 0, AUX_SC1);
                if (__all(((p0.y ^ tag) | (p0.w ^ tag) | (p1.y ^ tag) | (p1.w ^ tag)) == 0)) break;
                __builtin_amdgcn_s_sleep(8);
                asm volatile("" ::: "memory");
            }
            float ya = __uint_as_float(p0.x) + (on ? __uint_as_float(p1.x) : 0.0f), yb = __uint_as_float(p0.z) + (on ? __uint_as_float(p1.z) : 0.0f);
            ya += dppf<0x122>(ya); ya += dppf<0x124>(ya); ya += dppf<0x128>(ya); ya = sum32(sum16(ya));
            yb += dppf<0x122>(yb); yb += dppf<0x124>(yb); yb += dppf<0x128>(yb); yb = sum32(sum16(yb));
            const float y0 = ya, y1 = yb, y2 = dppf<0x130>(ya), y3 = dppf<0x130>(yb);
            const float rs = rbf(__builtin_amdgcn_rcpf(1.0f + fexp(-rsel)));
            lds_wait_ge(lds, C_ROWF, 1);
            const f4 rv = *(const f4*)ctl(lds, C_ROWV);
            const float o0 = rv[0] + rbf(rbf(y0) * rs), o1 = rv[1] + rbf(rbf(y1) * rs), o2 = rv[2] + rbf(rbf(y2) * rs), o3 = rv[3] + rbf(rbf(y3) * rs);
            if (lane == 0) __builtin_amdgcn_raw_buffer_store_b64((v2u){pkbf(o0, o1), pkbf(o2, o3)}, rout, idx * 2048 + cg * 8, 0, 0);
        }
    }
}

extern "C" void kernel_launch(void* const* d_in, const int* in_sizes, int n_in,
                              void* d_out, int out_size, void* d_ws, size_t ws_size,
                              hipStream_t stream) {
    KArgs a;
    a.x = (const u16*)d_in[0]; a.lar = (const u16*)d_in[1]; a.aim = (const u16*)d_in[2]; a.cre = (const u16*)d_in[3]; a.cim = (const u16*)d_in[4];
    a.ldt = (const u16*)d_in[5]; a.dsk = (const u16*)d_in[6]; a.wr = (const u16*)d_in[8];
    a.wg = (const u16*)d_in[9]; a.wu = (const u16*)d_in[10]; a.wd = (const u16*)d_in[11];
    a.out = (u16*)d_out; a.ws = (char*)d_ws;
    k_main<<<256, 1024, 0, stream>>>(a);
}
```
